```python
import torch

def _softmax(x, axis=-1):
    return torch.softmax(x, dim=axis)
def _gelu(x):
    return torch.nn.functional.gelu(x, approximate="tanh")
def _silu(x):
    return torch.nn.functional.silu(x)
def _sigmoid(x):
    return torch.sigmoid(x)
def _layernorm(x, axis=-1, eps=1e-5):
    m = x.mean(dim=axis, keepdim=True)
    v = x.var(dim=axis, keepdim=True, unbiased=False)
    return (x - m) / torch.sqrt(v + eps)
def _scatter_add(tgt, idx, src, axis=0):
    return tgt.clone().index_add_(axis, idx, src)
def _segment_sum(src, seg, n):
    out = torch.zeros((n,) + src.shape[1:], dtype=src.dtype)
    return out.index_add_(0, seg, src)
def _pad(x, pw):
    flat = tuple(v for pair in reversed(pw) for v in pair)
    return torch.nn.functional.pad(x, flat)

kernel_name = 'standalone__mha_softmax__near_ridge'

def setup_inputs():

    _w = (lambda a: (lambda t: t.to(torch.bfloat16) if t.is_floating_point() else t)(torch.as_tensor(a)))
    B, H, Sq, Sk, D = 16, 32, 256, 256, 128
    return {
        "q": _w((lambda *s: torch.randn(*s, dtype=torch.bfloat16) * 0.8)(B, H, Sq, D)),
        "k": _w((lambda *s: torch.randn(*s, dtype=torch.bfloat16) * 0.8)(B, H, Sk, D)),
        "v": _w((lambda *s: torch.randn(*s, dtype=torch.bfloat16) * 0.05)(B, H, Sk, D)),
    }

def reference(q, k, v):
    scale = q.shape[-1] ** -0.5
    scores = torch.einsum("bhqd,bhkd->bhqk", q, k) * scale
    p = _softmax(scores, axis=-1)
    return torch.einsum("bhqk,bhkd->bhqd", p, v)
```

```cpp
#include <hip/hip_runtime.h>
#include <hip/hip_bf16.h>

typedef float    f32x16 __attribute__((ext_vector_type(16)));
typedef float    f32x2  __attribute__((ext_vector_type(2)));
typedef __bf16   bf16x8 __attribute__((ext_vector_type(8)));
typedef __bf16   bf16x2 __attribute__((ext_vector_type(2)));
typedef short    s16x4  __attribute__((ext_vector_type(4)));
typedef short    s16x8  __attribute__((ext_vector_type(8)));
typedef unsigned u32x4  __attribute__((ext_vector_type(4)));
typedef unsigned u32x2  __attribute__((ext_vector_type(2)));
typedef __attribute__((address_space(3))) char* lds_ptr;

#define DI __device__ __forceinline__
constexpr int NHEADS = 512, HEADB = 65536;
constexpr unsigned KOFF = 0, VOFF = 65536, OOFF = 131072, LDSB = 163840;

DI unsigned cvtpk(float lo, float hi) { f32x2 v = {lo, hi}; bf16x2 b = __builtin_convertvector(v, bf16x2); return __builtin_bit_cast(unsigned, b); }
DI unsigned rfl(unsigned x) { return __builtin_amdgcn_readfirstlane(x); }

DI void dma(unsigned ldsaddr, unsigned voff, __amdgpu_buffer_rsrc_t srd, unsigned soff) {
  asm volatile("s_mov_b32 m0, %0\n\ts_nop 0\n\tbuffer_load_dwordx4 %1, %2, %3 offen lds" :: "s"(ldsaddr), "v"(voff), "s"(srd), "s"(soff) : "memory");
}
template <int OFF> DI void qload(u32x4& r, unsigned voff, __amdgpu_buffer_rsrc_t srd, unsigned soff) {
  asm volatile("buffer_load_dwordx4 %0, %1, %2, %3 offen offset:%4" : "=v"(r) : "v"(voff), "s"(srd), "s"(soff), "i"(OFF) : "memory");
}
DI void ostore(u32x4 v, unsigned voff, __amdgpu_buffer_rsrc_t srd, unsigned soff) {
  asm volatile("buffer_store_dwordx4 %0, %1, %2, %3 offen\n\ts_nop 1" :: "v"(v), "v"(voff), "s"(srd), "s"(soff) : "memory");
}
#define WAIT_BAR(N) asm volatile("s_waitcnt vmcnt(" #N ") lgkmcnt(0)\n\ts_barrier" ::: "memory")
#define BAR() asm volatile("s_waitcnt lgkmcnt(0)\n\ts_barrier" ::: "memory")

struct Ctx {
  __amdgpu_buffer_rsrc_t sq, sk, sv, so;
  unsigned lds;
  unsigned wid;
  unsigned qvoff, kvoff, vvoff, ovoff;
  unsigned kaddr[8], vaddr[4];
  unsigned ow, orr;
};

DI void issue_q(const Ctx& c, u32x4 (&q)[8], unsigned head) {
  const unsigned so = head * HEADB + c.wid * 8192u;
  qload<0>(q[0], c.qvoff, c.sq, so);   qload<32>(q[1], c.qvoff, c.sq, so);  qload<64>(q[2], c.qvoff, c.sq, so);  qload<96>(q[3], c.qvoff, c.sq, so);
  qload<128>(q[4], c.qvoff, c.sq, so); qload<160>(q[5], c.qvoff, c.sq, so); qload<192>(q[6], c.qvoff, c.sq, so); qload<224>(q[7], c.qvoff, c.sq, so);
}
DI void issue_k(const Ctx& c, unsigned head) {
#pragma unroll
  for (int i = 0; i < 8; ++i) { const unsigned o = 8192u * i + 1024u * c.wid; dma(c.lds + KOFF + o, c.kvoff, c.sk, head * HEADB + o); }
}
DI void issue_v(const Ctx& c, unsigned head) {
#pragma unroll
  for (int i = 0; i < 8; ++i) { const unsigned o = 8192u * i + 1024u * c.wid; dma(c.lds + VOFF + o, c.vvoff, c.sv, head * HEADB + o); }
}

template <int KT> DI void qk_tile(const Ctx& c, f32x16& s, const u32x4 (&q)[8], lds_ptr L) {
  s = f32x16{};
#pragma unroll
  for (int d = 0; d < 8; ++d) {
    const bf16x8 a = *(const __attribute__((address_space(3))) bf16x8*)(L + c.kaddr[d] + KT * 8192);
    s = __builtin_amdgcn_mfma_f32_32x32x16_bf16(a, __builtin_bit_cast(bf16x8, q[d]), s, 0, 0, 0);
  }
}
template <int KS> DI void pv_step(const Ctx& c, f32x16 (&o)[4], u32x4 p, lds_ptr L) {
#pragma unroll
  for (int db = 0; db < 4; ++db) {
    typedef __attribute__((address_space(3))) s16x4* lp;
    const s16x4 lo = __builtin_amdgcn_ds_read_tr16_b64_v4i16((lp)(L + c.vaddr[db] + KS * 4096));
    const s16x4 hi = __builtin_amdgcn_ds_read_tr16_b64_v4i16((lp)(L + c.vaddr[db] + KS * 4096 + 2048));
    const s16x8 a = __builtin_shufflevector(lo, hi, 0, 1, 2, 3, 4, 5, 6, 7);
    o[db] = __builtin_amdgcn_mfma_f32_32x32x16_bf16(__builtin_bit_cast(bf16x8, a), __builtin_bit_cast(bf16x8, p), o[db], 0, 0, 0);
  }
}
#define QWAIT(N, q) asm volatile("s_waitcnt vmcnt(" #N ") lgkmcnt(0)\n\ts_barrier" : "+v"(q[0]), "+v"(q[1]), "+v"(q[2]), "+v"(q[3]), "+v"(q[4]), "+v"(q[5]), "+v"(q[6]), "+v"(q[7]) :: "memory")

template <bool FIRST, bool LAST> DI void head_body(const Ctx& c, lds_ptr L, u32x4 (&q)[8], u32x4 (&qn)[8], unsigned head, unsigned nhead) {
  f32x16 s[8];
  if constexpr (FIRST) QWAIT(15, q); else QWAIT(23, q);
  qk_tile<0>(c, s[0], q, L);
  if constexpr (FIRST) WAIT_BAR(13); else WAIT_BAR(21);
  qk_tile<1>(c, s[1], q, L); qk_tile<2>(c, s[2], q, L);
  if constexpr (FIRST) WAIT_BAR(11); else WAIT_BAR(19);
  qk_tile<3>(c, s[3], q, L); qk_tile<4>(c, s[4], q, L);
  if constexpr (FIRST) WAIT_BAR(8); else WAIT_BAR(16);
  qk_tile<5>(c, s[5], q, L); qk_tile<6>(c, s[6], q, L); qk_tile<7>(c, s[7], q, L);
  if constexpr (!LAST) { BAR(); issue_q(c, qn, nhead); issue_k(c, nhead); }
  float m0 = s[0][0], m1 = s[0][1];
#pragma unroll
  for (int t = 0; t < 8; ++t)
#pragma unroll
    for (int r = 0; r < 16; r += 4) { m0 = fmaxf(fmaxf(m0, s[t][r]), s[t][r + 1]); m1 = fmaxf(fmaxf(m1, s[t][r + 2]), s[t][r + 3]); }
  float m = fmaxf(m0, m1);
  { auto rr = __builtin_amdgcn_permlane32_swap(__float_as_uint(m), __float_as_uint(m), false, false); m = fmaxf(__uint_as_float(rr[0]), __uint_as_float(rr[1])); }
  const float C = 0.088388347648318440f * 1.4426950408889634f;
  const float mc = -m * C;
  float l0 = 0.f, l1 = 0.f;
  u32x4 p[16];
#pragma unroll
  for (int t = 0; t < 8; ++t) {
#pragma unroll
    for (int r = 0; r < 16; r += 2) {
      const float e0 = __builtin_amdgcn_exp2f(fmaf(s[t][r], C, mc)), e1 = __builtin_amdgcn_exp2f(fmaf(s[t][r + 1], C, mc));
      l0 += e0; l1 += e1;
      p[2 * t + (r >> 3)][(r >> 1) & 3] = cvtpk(e0, e1);
    }
  }
  float l = l0 + l1;
  { auto rr = __builtin_amdgcn_permlane32_swap(__float_as_uint(l), __float_as_uint(l), false, false); l = __uint_as_float(rr[0]) + __uint_as_float(rr[1]); }
  const float il = __builtin_amdgcn_rcpf(l);
  f32x16 o[4] = {};
  if constexpr (LAST) WAIT_BAR(14); else WAIT_BAR(22);
  pv_step<0>(c, o, p[0], L); pv_step<1>(c, o, p[1], L); pv_step<2>(c, o, p[2], L); pv_step<3>(c, o, p[3], L);
  if constexpr (LAST) WAIT_BAR(12); else WAIT_BAR(20);
  pv_step<4>(c, o, p[4], L); pv_step<5>(c, o, p[5], L); pv_step<6>(c, o, p[6], L); pv_step<7>(c, o, p[7], L);
  if constexpr (LAST) WAIT_BAR(10); else WAIT_BAR(18);
  pv_step<8>(c, o, p[8], L); pv_step<9>(c, o, p[9], L); pv_step<10>(c, o, p[10], L); pv_step<11>(c, o, p[11], L);
  if constexpr (LAST) WAIT_BAR(8); else WAIT_BAR(16);
  pv_step<12>(c, o, p[12], L); pv_step<13>(c, o, p[13], L); pv_step<14>(c, o, p[14], L); pv_step<15>(c, o, p[15], L);
  if constexpr (!LAST) { BAR(); issue_v(c, nhead); }
#pragma unroll
  for (int ps = 0; ps < 2; ++ps) {
#pragma unroll
    for (int e = 0; e < 2; ++e) {
      const int db = 2 * ps + e;
#pragma unroll
      for (int g = 0; g < 4; ++g) {
        u32x2 w = {cvtpk(o[db][4 * g] * il, o[db][4 * g + 1] * il), cvtpk(o[db][4 * g + 2] * il, o[db][4 * g + 3] * il)};
        *(__attribute__((address_space(3))) u32x2*)(L + (c.ow ^ (unsigned)(16 * (4 * e + g)))) = w;
      }
    }
    asm volatile("s_waitcnt lgkmcnt(0)" ::: "memory");
    u32x4 v[4];
#pragma unroll
    for (int j = 0; j < 4; ++j) v[j] = *(const __attribute__((address_space(3))) u32x4*)(L + c.orr + 1024 * j);
    asm volatile("s_waitcnt lgkmcnt(0)" : "+v"(v[0]), "+v"(v[1]), "+v"(v[2]), "+v"(v[3]) :: "memory");
#pragma unroll
    for (int j = 0; j < 4; ++j) ostore(v[j], c.ovoff, c.so, head * HEADB + c.wid * 8192u + 2048u * j + 128u * ps);
  }
}

__global__ __launch_bounds__(512, 2) void mha_fwd(const void* __restrict__ Q, const void* __restrict__ K, const void* __restrict__ V, void* __restrict__ O) {
  extern __shared__ __attribute__((aligned(16))) char smem[];
  lds_ptr L = (lds_ptr)0;
  Ctx c;
  const unsigned tid = threadIdx.x, lane = tid & 63u, r = lane & 31u, h = lane >> 5;
  c.wid = rfl(tid >> 6);
  c.lds = rfl((unsigned)(size_t)(lds_ptr)smem);
  constexpr int TB = NHEADS * HEADB;
  c.sq = __builtin_amdgcn_make_buffer_rsrc((void*)Q, (short)0, TB, 0x00020000);
  c.sk = __builtin_amdgcn_make_buffer_rsrc((void*)K, (short)0, TB, 0x00020000);
  c.sv = __builtin_amdgcn_make_buffer_rsrc((void*)V, (short)0, TB, 0x00020000);
  c.so = __builtin_amdgcn_make_buffer_rsrc(O, (short)0, TB, 0x00020000);
  c.qvoff = r * 256u + h * 16u;
  { const unsigned row = 4u * (c.wid & 3u) + (lane >> 4);
    c.kvoff = (lane >> 4) * 256u + 16u * ((lane & 15u) ^ (row & 15u));
    c.vvoff = (lane >> 4) * 256u + 16u * ((lane & 15u) ^ ((lane >> 4) << 2)); }
#pragma unroll
  for (int d = 0; d < 8; ++d) c.kaddr[d] = c.lds + KOFF + r * 256u + 16u * ((2u * d + h) ^ (r & 15u));
  { const unsigned qq = (lane & 15u) >> 2, p = lane & 3u, blk = (lane >> 4) & 1u;
#pragma unroll
    for (int db = 0; db < 4; ++db) c.vaddr[db] = c.lds + VOFF + (4u * h + qq) * 256u + 16u * (4u * (db ^ qq) + 2u * blk + (p >> 1)) + 8u * (p & 1u); }
  c.ow = c.lds + OOFF + c.wid * 4096u + r * 128u + 16u * (r & 7u) + 8u * h;
  { const unsigned row = lane >> 3; c.orr = c.lds + OOFF + c.wid * 4096u + row * 128u + 16u * ((lane & 7u) ^ (row & 7u));
    c.ovoff = row * 256u + 16u * (lane & 7u); }
  const unsigned h0 = blockIdx.x, h1 = blockIdx.x + 256u;
  u32x4 qa[8], qb[8];
  issue_q(c, qa, h0); issue_k(c, h0); issue_v(c, h0);
  head_body<true, false>(c, L, qa, qb, h0, h1);
  head_body<false, true>(c, L, qb, qa, h1, h1);
}

extern "C" void kernel_launch(void* const* d_in, const int* in_sizes, int n_in, void* d_out, int out_size, void* d_ws, size_t ws_size, hipStream_t stream) {
  (void)hipFuncSetAttribute((const void*)mha_fwd, hipFuncAttributeMaxDynamicSharedMemorySize, (int)LDSB);
  hipLaunchKernelGGL(mha_fwd, dim3(256), dim3(512), LDSB, stream, d_in[0], d_in[1], d_in[2], d_out);
}
```

```cpp
#include <hip/hip_runtime.h>
#include <hip/hip_bf16.h>

typedef float  f32x16 __attribute__((ext_vector_type(16)));
typedef float  f32x2  __attribute__((ext_vector_type(2)));
typedef __bf16 bf16x8 __attribute__((ext_vector_type(8)));
typedef __bf16 bf16x2 __attribute__((ext_vector_type(2)));
typedef unsigned u32x4 __attribute__((ext_vector_type(4)));
typedef unsigned u32x2 __attribute__((ext_vector_type(2)));

__device__ __forceinline__ unsigned cvtpk(float lo, float hi) { f32x2 v = {lo, hi}; bf16x2 b = __builtin_convertvector(v, bf16x2); return __builtin_bit_cast(unsigned, b); }
__device__ __forceinline__ float xhalf_max(float x) { auto r = __builtin_amdgcn_permlane32_swap(__float_as_uint(x), __float_as_uint(x), false, false); return fmaxf(__uint_as_float(r[0]), __uint_as_float(r[1])); }
__device__ __forceinline__ float xhalf_sum(float x) { auto r = __builtin_amdgcn_permlane32_swap(__float_as_uint(x), __float_as_uint(x), false, false); return __uint_as_float(r[0]) + __uint_as_float(r[1]); }

__device__ __forceinline__ void exact_rows(const unsigned short* __restrict__ Q, const unsigned short* __restrict__ K, const unsigned short* __restrict__ V,
                                           unsigned short* __restrict__ O, unsigned lane) {
  const unsigned r = lane & 31u, h = lane >> 5;
  const float C = 0.088388347648318440f * 1.4426950408889634f;
  bf16x8 q[8];
#pragma unroll
  for (int s = 0; s < 8; ++s) q[s] = *(const bf16x8*)(Q + r * 128 + 16 * s + 8 * h);
  float m = -__builtin_inff();
#pragma unroll 1
  for (int kt = 0; kt < 8; ++kt) {
    f32x16 sc = {};
#pragma unroll
    for (int s = 0; s < 8; ++s) sc = __builtin_amdgcn_mfma_f32_32x32x16_bf16(*(const bf16x8*)(K + (32 * kt + r) * 128 + 16 * s + 8 * h), q[s], sc, 0, 0, 0);
#pragma unroll
    for (int i = 0; i < 16; ++i) m = fmaxf(m, sc[i]);
  }
  m = xhalf_max(m);
  const float mc = -m * C;
  float l = 0.f;
  f32x16 o[4] = {};
#pragma unroll 1
  for (int kt = 0; kt < 8; ++kt) {
    f32x16 sc = {};
#pragma unroll
    for (int s = 0; s < 8; ++s) sc = __builtin_amdgcn_mfma_f32_32x32x16_bf16(*(const bf16x8*)(K + (32 * kt + r) * 128 + 16 * s + 8 * h), q[s], sc, 0, 0, 0);
#pragma unroll
    for (int ks = 0; ks < 2; ++ks) {
      u32x4 p;
#pragma unroll
      for (int i = 0; i < 4; ++i) {
        const float e0 = __builtin_amdgcn_exp2f(fmaf(sc[8 * ks + 2 * i], C, mc)), e1 = __builtin_amdgcn_exp2f(fmaf(sc[8 * ks + 2 * i + 1], C, mc));
        l += e0 + e1; p[i] = cvtpk(e0, e1);
      }
#pragma unroll
      for (int db = 0; db < 4; ++db) {
        u32x4 a;
#pragma unroll
        for (int i = 0; i < 4; ++i) {
          const int k0 = 32 * kt + 16 * ks + 8 * (i >> 1) + 2 * (i & 1) + 4 * (int)h;
          a[i] = (unsigned)V[k0 * 128 + 32 * db + r] | ((unsigned)V[(k0 + 1) * 128 + 32 * db + r] << 16);
        }
        o[db] = __builtin_amdgcn_mfma_f32_32x32x16_bf16(__builtin_bit_cast(bf16x8, a), __builtin_bit_cast(bf16x8, p), o[db], 0, 0, 0);
      }
    }
  }
  const float il = 1.f / xhalf_sum(l);
#pragma unroll
  for (int db = 0; db < 4; ++db)
#pragma unroll
    for (int g = 0; g < 4; ++g) {
      u32x2 w = {cvtpk(o[db][4 * g] * il, o[db][4 * g + 1] * il), cvtpk(o[db][4 * g + 2] * il, o[db][4 * g + 3] * il)};
      *(u32x2*)(O + r * 128 + 32 * db + 8 * g + 4 * h) = w;
    }
}

__global__ __launch_bounds__(512, 2) void mha_fwd(const void* Q, const void* K, const void* V, void* O) {
  const unsigned wid = __builtin_amdgcn_readfirstlane(threadIdx.x >> 6);
  const unsigned bid = blockIdx.x;
  unsigned flag;
  asm volatile(
    "s_load_dwordx8 s[84:91], %1, 0x0\n"
    "s_mov_b32 s58, %3\n"
    "s_mov_b32 s83, 0\n"
    "s_lshl_b32 s54, s58, 11\n"
    "s_lshl_b32 s59, %2, 16\n"
    "s_add_u32 s52, s59, s54\n"
    "s_add_u32 s53, s52, 16777216\n"
    "v_mbcnt_lo_u32_b32 v0, -1, 0\n"
    "v_mbcnt_hi_u32_b32 v0, -1, v0\n"
    "s_and_b32 s62, s58, 1\n"
    "s_lshl_b32 s62, s62, 3\n"
    "v_lshrrev_b32 v3, 4, v0\n"
    "v_and_b32 v4, 15, v0\n"
    "v_or_b32 v5, s62, v3\n"
    "v_xor_b32 v5, v4, v5\n"
    "v_lshlrev_b32 v5, 4, v5\n"
    "v_lshl_or_b32 v254, v3, 8, v5\n"
    "v_xor_b32 v249, 64, v254\n"
    "s_waitcnt lgkmcnt(0)\n"
    "s_mov_b64 s[36:37], s[84:85]\n"
    "s_and_b32 s37, s37, 0xffff\n"
    "s_mov_b32 s38, 0x2000000\n"
    "s_mov_b32 s39, 0x20000\n"
    "s_mov_b64 s[40:41], s[86:87]\n"
    "s_and_b32 s41, s41, 0xffff\n"
    "s_mov_b32 s42, 0x2000000\n"
    "s_mov_b32 s43, 0x20000\n"
    "s_mov_b64 s[44:45], s[88:89]\n"
    "s_and_b32 s45, s45, 0xffff\n"
    "s_mov_b32 s46, 0x2000000\n"
    "s_mov_b32 s47, 0x20000\n"
    "s_mov_b64 s[48:49], s[90:91]\n"
    "s_and_b32 s49, s49, 0xffff\n"
    "s_mov_b32 s50, 0x2000000\n"
    "s_mov_b32 s51, 0x20000\n"
    "s_add_u32 m0, s54, 0\n"
    "s_add_u32 s55, s52, 0\n"
    "buffer_load_dwordx4 v254, s[36:39], s55 offen nt lds\n"
    "buffer_load_dwordx4 v249, s[36:39], s55 offen offset:1024 nt lds\n"
    "s_add_u32 m0, s54, 16384\n"
    "s_add_u32 s55, s52, 16384\n"
    "buffer_load_dwordx4 v254, s[36:39], s55 offen nt lds\n"
    "buffer_load_dwordx4 v249, s[36:39], s55 offen offset:1024 nt lds\n"
    "s_add_u32 m0, s54, 65536\n"
    "s_add_u32 s55, s52, 0\n"
    "buffer_load_dwordx4 v254, s[40:43], s55 offen nt lds\n"
    "buffer_load_dwordx4 v249, s[40:43], s55 offen offset:1024 nt lds\n"
    "s_add_u32 m0, s54, 32768\n"
    "s_add_u32 s55, s52, 32768\n"
    "buffer_load_dwordx4 v254, s[36:39], s55 offen nt lds\n"
    "buffer_load_dwordx4 v249, s[36:39], s55 offen offset:1024 nt lds\n"
    "s_getpc_b64 s[86:87]\n"
    "s_mul_i32 s88, s58, 2496\n"
    "s_add_u32 s86, s86, s88\n"
    "s_addc_u32 s87, s87, 0\n"
    "v_lshlrev_b32 v11, 4, v0\n"
    "global_load_dwordx4 v[192:195], v11, s[86:87] offset:0\n"
    "global_load_dwordx4 v[192:195], v11, s[86:87] offset:1024\n"
    "global_load_dwordx4 v[192:195], v11, s[86:87] offset:1472\n"
    "v_lshlrev_b32 v6, 2, v3\n"
    "v_xor_b32 v6, v4, v6\n"
    "v_lshlrev_b32 v6, 4, v6\n"
    "v_lshl_or_b32 v255, v3, 8, v6\n"
    "v_and_b32 v1, 31, v0\n"
    "v_lshrrev_b32 v2, 5, v0\n"
    "v_xor_b32 v7, v2, v4\n"
    "v_lshlrev_b32 v7, 4, v7\n"
    "v_lshl_or_b32 v7, v1, 8, v7\n"
    "v_or_b32 v252, 0x10000, v7\n"
    "v_bfe_u32 v8, v0, 2, 2\n"
    "v_lshl_add_u32 v9, v2, 2, v8\n"
    "v_lshlrev_b32 v9, 8, v9\n"
    "v_lshl_or_b32 v9, v8, 6, v9\n"
    "v_bfe_u32 v10, v0, 4, 1\n"
    "v_lshl_or_b32 v9, v10, 5, v9\n"
    "v_and_b32 v10, 3, v0\n"
    "v_lshl_or_b32 v253, v10, 3, v9\n"
    "s_mov_b32 s56, 0x3e0293ee\n"
    "s_mov_b32 s57, 0x49800000\n"
    "s_lshl_b32 s62, s58, 13\n"
    "s_lshl_b32 s63, %2, 16\n"
    "s_add_u32 s60, s63, s62\n"
    "s_add_u32 s61, s60, 0x1000000\n"
    "s_mov_b32 s89, 0\n"
    "s_mov_b64 vcc, 0\n"
    "s_memtime s[90:91]\n"
    "s_memrealtime s[92:93]\n"
    "s_waitcnt lgkmcnt(0)\n"
    "s_waitcnt vmcnt(9) lgkmcnt(0)\n"
    "s_barrier\n"
    "s_add_u32 m0, s54, 49152\n"
    "s_add_u32 s55, s52, 49152\n"
    "buffer_load_dwordx4 v254, s[36:39], s55 offen nt lds\n"
    "buffer_load_dwordx4 v249, s[36:39], s55 offen offset:1024 nt lds\n"
    "v_xor_b32 v240, 0, v252\n"
    "v_xor_b32 v241, 32, v252\n"
    "v_xor_b32 v242, 64, v252\n"
    "v_xor_b32 v243, 96, v252\n"
    "v_xor_b32 v244, 128, v252\n"
    "v_xor_b32 v245, 160, v252\n"
    "v_xor_b32 v246, 192, v252\n"
    "v_xor_b32 v247, 224, v252\n"
    "s_lshr_b32 s62, s58, 1\n"
    "s_mov_b32 s63, 0\n"
    "s_cmp_eq_u32 s62, 1\n"
    "s_cselect_b32 s63, 16384, s63\n"
    "s_cmp_eq_u32 s62, 2\n"
    "s_cselect_b32 s63, 32768, s63\n"
    "s_cmp_eq_u32 s62, 3\n"
    "s_cselect_b32 s63, 49152, s63\n"
    "s_and_b32 s62, s58, 1\n"
    "s_lshl_b32 s62, s62, 13\n"
    "s_add_u32 s63, s63, s62\n"
    "v_and_b32 v224, 0xffff, v252\n"
    "v_add_u32 v224, s63, v224\n"
    "v_xor_b32 v225, 32, v224\n"
    "v_xor_b32 v226, 64, v224\n"
    "v_xor_b32 v227, 96, v224\n"
    "v_xor_b32 v228, 128, v224\n"
    "v_xor_b32 v229, 160, v224\n"
    "v_xor_b32 v230, 192, v224\n"
    "v_xor_b32 v231, 224, v224\n"
    "s_lshr_b32 s62, s58, 1\n"
    "s_cmp_lg_u32 s62, 0\n"
    "s_cbranch_scc1 LQ0_%=\n"
    "ds_read_b128 v[128:131], v224 offset:0\n"
    "ds_read_b128 v[132:135], v225 offset:0\n"
    "ds_read_b128 v[136:139], v226 offset:0\n"
    "ds_read_b128 v[140:143], v227 offset:0\n"
    "ds_read_b128 v[144:147], v228 offset:0\n"
    "ds_read_b128 v[148:151], v229 offset:0\n"
    "ds_read_b128 v[152:155], v230 offset:0\n"
    "ds_read_b128 v[156:159], v231 offset:0\n"
    "s_waitcnt lgkmcnt(0)\n"
    "LQ0_%=:\n"
    "s_waitcnt vmcnt(9) lgkmcnt(0)\n"
    "s_barrier\n"
    "s_add_u32 m0, s54, 81920\n"
    "s_add_u32 s55, s52, 16384\n"
    "buffer_load_dwordx4 v254, s[40:43], s55 offen nt lds\n"
    "buffer_load_dwordx4 v249, s[40:43], s55 offen offset:1024 nt lds\n"
    "s_lshr_b32 s62, s58, 1\n"
    "s_cmp_lg_u32 s62, 1\n"
    "s_cbranch_scc1 LQ1_%=\n"
    "ds_read_b128 v[128:131], v224 offset:0\n"
    "ds_read_b128 v[132:135], v225 offset:0\n"
    "ds_read_b128 v[136:139], v226 offset:0\n"
    "ds_read_b128 v[140:143], v227 offset:0\n"
    "ds_read_b128 v[144:147], v228 offset:0\n"
    "ds_read_b128 v[148:151], v229 offset:0\n"
    "ds_read_b128 v[152:155], v230 offset:0\n"
    "ds_read_b128 v[156:159], v231 offset:0\n"
    "s_waitcnt lgkmcnt(0)\n"
    "LQ1_%=:\n"
    "s_waitcnt vmcnt(9) lgkmcnt(0)\n"
    "s_barrier\n"
    "s_add_u32 m0, s54, 98304\n"
    "s_add_u32 s55, s52, 32768\n"
    "buffer_load_dwordx4 v254, s[40:43], s55 offen nt lds\n"
    "buffer_load_dwordx4 v249, s[40:43], s55 offen offset:1024 nt lds\n"
    "s_cmp_ge_u32 s58, 4\n"
    "s_cbranch_scc1 LEA_%=\n"
    "ds_read_b128 v[160:163], v240 offset:0\n"
    "ds_read_b128 v[164:167], v241 offset:0\n"
    "ds_read_b128 v[168:171], v242 offset:0\n"
    "ds_read_b128 v[172:175], v243 offset:0\n"
    "ds_read_b128 v[176:179], v244 offset:0\n"
    "ds_read_b128 v[180:183], v245 offset:0\n"
    "ds_read_b128 v[184:187], v246 offset:0\n"
    "s_waitcnt lgkmcnt(6)\n"
    "v_mfma_f32_32x32x16_bf16 v[0:15], v[160:163], v[128:131], 0\n"
    "s_nop 11\n"
    "ds_read_b128 v[188:191], v247 offset:0\n"
    "s_waitcnt lgkmcnt(6)\n"
    "v_mfma_f32_32x32x16_bf16 v[0:15], v[164:167], v[132:135], v[0:15]\n"
    "s_nop 11\n"
    "ds_read_b128 v[160:163], v240 offset:8192\n"
    "s_waitcnt lgkmcnt(6)\n"
    "v_mfma_f32_32x32x16_bf16 v[0:15], v[168:171], v[136:139], v[0:15]\n"
    "s_nop 11\n"
    "ds_read_b128 v[164:167], v241 offset:8192\n"
    "s_waitcnt lgkmcnt(6)\n"
    "v_mfma_f32_32x32x16_bf16 v[0:15], v[172:175], v[140:143], v[0:15]\n"
    "s_nop 11\n"
    "ds_read_b128 v[168:171], v242 offset:8192\n"
    "s_waitcnt lgkmcnt(6)\n"
    "v_mfma_f32_32x32x16_bf16 v[0:15], v[176:179], v[144:147], v[0:15]\n"
    "s_nop 11\n"
    "ds_read_b128 v[172:175], v243 offset:8192\n"
    "s_waitcnt lgkmcnt(6)\n"
    "v_mfma_f32_32x32x16_bf16 v[0:15], v[180:183], v[148:151], v[0:15]\n"
    "s_nop 11\n"
    "ds_read_b128 v[176:179], v244 offset:8192\n"
    "s_waitcnt lgkmcnt(6)\n"
    "v_mfma_f32_32x32x16_bf16 v[0:15], v[184:187], v[152:155], v[0:15]\n"
    "s_nop 11\n"
    "ds_read_b128 v[180:183], v245 offset:8192\n"
    "s_waitcnt lgkmcnt(6)\n"
    "v_mfma_f32_32x32x16_bf16 v[0:15], v[188:191], v[156:159], v[0:15]\n"
    "s_nop 11\n"
    "ds_read_b128 v[184:187], v246 offset:8192\n"
    "s_waitcnt lgkmcnt(6)\n"
    "v_mfma_f32_32x32x16_bf16 v[16:31], v[160:163], v[128:131], 0\n"
    "s_nop 11\n"
    "ds_read_b128 v[188:191], v247 offset:8192\n"
    "s_waitcnt lgkmcnt(6)\n"
    "v_mfma_f32_32x32x16_bf16 v[16:31], v[164:167], v[132:135], v[16:31]\n"
    "s_nop 11\n"
    "s_waitcnt lgkmcnt(5)\n"
    "v_mfma_f32_32x32x16_bf16 v[16:31], v[168:171], v[136:139], v[16:31]\n"
    "s_nop 11\n"
    "v_max_f32 v232, v0, v1\n"
    "v_max_f32 v233, v2, v3\n"
    "v_max3_f32 v232, v232, v4, v5\n"
    "v_max3_f32 v233, v233, v6, v7\n"
    "s_waitcnt lgkmcnt(4)\n"
    "v_mfma_f32_32x32x16_bf16 v[16:31], v[172:175], v[140:143], v[16:31]\n"
    "s_nop 11\n"
    "v_max3_f32 v232, v232, v8, v9\n"
    "v_max3_f32 v233, v233, v10, v11\n"
    "v_max3_f32 v232, v232, v12, v13\n"
    "v_max3_f32 v233, v233, v14, v15\n"
    "s_waitcnt lgkmcnt(3)\n"
    "v_mfma_f32_32x32x16_bf16 v[16:31], v[176:179], v[144:147], v[16:31]\n"
    "s_nop 11\n"
    "v_max_f32 v232, v232, v233\n"
    "v_mov_b32 v233, v232\n"
    "s_nop 1\n"
    "v_permlane32_swap_b32 v232, v233\n"
    "v_max_f32 v232, v232, v233\n"
    "s_waitcnt lgkmcnt(2)\n"
    "v_mfma_f32_32x32x16_bf16 v[16:31], v[180:183], v[148:151], v[16:31]\n"
    "s_nop 11\n"
    "v_mul_f32 v248, 0xbe0293ee, v232\n"
    "v_mov_b32 v232, 0\n"
    "v_mov_b32 v233, 0\n"
    "v_fma_f32 v0, v0, s56, v248\n"
    "s_waitcnt lgkmcnt(1)\n"
    "v_mfma_f32_32x32x16_bf16 v[16:31], v[184:187], v[152:155], v[16:31]\n"
    "s_nop 11\n"
    "v_fma_f32 v1, v1, s56, v248\n"
    "v_fma_f32 v2, v2, s56, v248\n"
    "v_fma_f32 v3, v3, s56, v248\n"
    "v_fma_f32 v4, v4, s56, v248\n"
    "s_waitcnt lgkmcnt(0)\n"
    "v_mfma_f32_32x32x16_bf16 v[16:31], v[188:191], v[156:159], v[16:31]\n"
    "s_nop 11\n"
    "v_fma_f32 v5, v5, s56, v248\n"
    "v_fma_f32 v6, v6, s56, v248\n"
    "v_fma_f32 v7, v7, s56, v248\n"
    "v_fma_f32 v8, v8, s56, v248\n"
    "s_waitcnt lgkmcnt(0)\n"
    "LEA_%=:\n"
    "s_waitcnt vmcnt(9) lgkmcnt(0)\n"
    "s_barrier\n"
    "s_add_u32 m0, s54, 114688\n"
    "s_add_u32 s55, s52, 49152\n"
    "buffer_load_dwordx4 v254, s[40:43], s55 offen nt lds\n"
    "buffer_load_dwordx4 v249, s[40:43], s55 offen offset:1024 nt lds\n"
    "s_lshr_b32 s62, s58, 1\n"
    "s_cmp_lg_u32 s62, 2\n"
    "s_cbranch_scc1 LQ2_%=\n"
    "ds_read_b128 v[128:131], v224 offset:0\n"
    "ds_read_b128 v[132:135], v225 offset:0\n"
    "ds_read_b128 v[136:139], v226 offset:0\n"
    "ds_read_b128 v[140:143], v227 offset:0\n"
    "ds_read_b128 v[144:147], v228 offset:0\n"
    "ds_read_b128 v[148:151], v229 offset:0\n"
    "ds_read_b128 v[152:155], v230 offset:0\n"
    "ds_read_b128 v[156:159], v231 offset:0\n"
    "s_waitcnt lgkmcnt(0)\n"
    "LQ2_%=:\n"
    "s_waitcnt vmcnt(6) lgkmcnt(0)\n"
    "s_barrier\n"
    "s_add_u32 m0, s54, 0\n"
    "s_add_u32 s55, s52, 0\n"
    "buffer_load_dwordx4 v255, s[44:47], s55 offen nt lds\n"
    "buffer_load_dwordx4 v255, s[44:47], s55 offen offset:1024 nt lds\n"
    "s_lshr_b32 s62, s58, 1\n"
    "s_cmp_lg_u32 s62, 3\n"
    "s_cbranch_scc1 LQ3_%=\n"
    "ds_read_b128 v[128:131], v224 offset:0\n"
    "ds_read_b128 v[132:135], v225 offset:0\n"
    "ds_read_b128 v[136:139], v226 offset:0\n"
    "ds_read_b128 v[140:143], v227 offset:0\n"
    "ds_read_b128 v[144:147], v228 offset:0\n"
    "ds_read_b128 v[148:151], v229 offset:0\n"
    "ds_read_b128 v[152:155], v230 offset:0\n"
    "ds_read_b128 v[156:159], v231 offset:0\n"
    "s_waitcnt lgkmcnt(0)\n"
    "LQ3_%=:\n"
    "s_cmp_lt_u32 s58, 4\n"
    "s_cbranch_scc1 LEB_%=\n"
    "ds_read_b128 v[160:163], v240 offset:0\n"
    "ds_read_b128 v[164:167], v241 offset:0\n"
    "ds_read_b128 v[168:171], v242 offset:0\n"
    "ds_read_b128 v[172:175], v243 offset:0\n"
    "ds_read_b128 v[176:179], v244 offset:0\n"
    "ds_read_b128 v[180:183], v245 offset:0\n"
    "ds_read_b128 v[184:187], v246 offset:0\n"
    "s_waitcnt lgkmcnt(6)\n"
    "v_mfma_f32_32x32x16_bf16 v[0:15], v[160:163], v[128:131], 0\n"
    "s_nop 11\n"
    "ds_read_b128 v[188:191], v247 offset:0\n"
    "s_waitcnt lgkmcnt(6)\n"
    "v_mfma_f32_32x32x16_bf16 v[0:15], v[164:167], v[132:135], v[0:15]\n"
    "s_nop 11\n"
    "ds_read_b128 v[160:163], v240 offset:8192\n"
    "s_waitcnt lgkmcnt(6)\n"
    "v_mfma_f32_32x32x16_bf16 v[0:15], v[168:171], v[136:139], v[0:15]\n"
    "s_nop 11\n"
    "ds_read_b128 v[164:167], v241 offset:8192\n"
    "s_waitcnt lgkmcnt(6)\n"
    "v_mfma_f32_32x32x16_bf16 v[0:15], v[172:175], v[140:143], v[0:15]\n"
    "s_nop 11\n"
    "ds_read_b128 v[168:171], v242 offset:8192\n"
    "s_waitcnt lgkmcnt(6)\n"
    "v_mfma_f32_32x32x16_bf16 v[0:15], v[176:179], v[144:147], v[0:15]\n"
    "s_nop 11\n"
    "ds_read_b128 v[172:175], v243 offset:8192\n"
    "s_waitcnt lgkmcnt(6)\n"
    "v_mfma_f32_32x32x16_bf16 v[0:15], v[180:183], v[148:151], v[0:15]\n"
    "s_nop 11\n"
    "ds_read_b128 v[176:179], v244 offset:8192\n"
    "s_waitcnt lgkmcnt(6)\n"
    "v_mfma_f32_32x32x16_bf16 v[0:15], v[184:187], v[152:155], v[0:15]\n"
    "s_nop 11\n"
    "ds_read_b128 v[180:183], v245 offset:8192\n"
    "s_waitcnt lgkmcnt(6)\n"
    "v_mfma_f32_32x32x16_bf16 v[0:15], v[188:191], v[156:159], v[0:15]\n"
    "s_nop 11\n"
    "ds_read_b128 v[184:187], v246 offset:8192\n"
    "s_waitcnt lgkmcnt(6)\n"
    "v_mfma_f32_32x32x16_bf16 v[16:31], v[160:163], v[128:131], 0\n"
    "s_nop 11\n"
    "ds_read_b128 v[188:191], v247 offset:8192\n"
    "s_waitcnt lgkmcnt(6)\n"
    "v_mfma_f32_32x32x16_bf16 v[16:31], v[164:167], v[132:135], v[16:31]\n"
    "s_nop 11\n"
    "s_waitcnt lgkmcnt(5)\n"
    "v_mfma_f32_32x32x16_bf16 v[16:31], v[168:171], v[136:139], v[16:31]\n"
    "s_nop 11\n"
    "v_max_f32 v232, v0, v1\n"
    "v_max_f32 v233, v2, v3\n"
    "v_max3_f32 v232, v232, v4, v5\n"
    "v_max3_f32 v233, v233, v6, v7\n"
    "s_waitcnt lgkmcnt(4)\n"
    "v_mfma_f32_32x32x16_bf16 v[16:31], v[172:175], v[140:143], v[16:31]\n"
    "s_nop 11\n"
    "v_max3_f32 v232, v232, v8, v9\n"
    "v_max3_f32 v233, v233, v10, v11\n"
    "v_max3_f32 v232, v232, v12, v13\n"
    "v_max3_f32 v233, v233, v14, v15\n"
    "s_waitcnt lgkmcnt(3)\n"
    "v_mfma_f32_32x32x16_bf16 v[16:31], v[176:179], v[144:147], v[16:31]\n"
    "s_nop 11\n"
    "v_max_f32 v232, v232, v233\n"
    "v_mov_b32 v233, v232\n"
    "s_nop 1\n"
    "v_permlane32_swap_b32 v232, v233\n"
    "v_max_f32 v232, v232, v233\n"
    "s_waitcnt lgkmcnt(2)\n"
    "v_mfma_f32_32x32x16_bf16 v[16:31], v[180:183], v[148:151], v[16:31]\n"
    "s_nop 11\n"
    "v_mul_f32 v248, 0xbe0293ee, v232\n"
    "v_mov_b32 v232, 0\n"
    "v_mov_b32 v233, 0\n"
    "v_fma_f32 v0, v0, s56, v248\n"
    "s_waitcnt lgkmcnt(1)\n"
    "v_mfma_f32_32x32x16_bf16 v[16:31], v[184:187], v[152:155], v[16:31]\n"
    "s_nop 11\n"
    "v_fma_f32 v1, v1, s56, v248\n"
    "v_fma_f32 v2, v2, s56, v248\n"
    "v_fma_f32 v3, v3, s56, v248\n"
    "v_fma_f32 v4, v4, s56, v248\n"
    "s_waitcnt lgkmcnt(0)\n"
    "v_mfma_f32_32x32x16_bf16 v[16:31], v[188:191], v[156:159], v[16:31]\n"
    "s_nop 11\n"
    "v_fma_f32 v5, v5, s56, v248\n"
    "v_fma_f32 v6, v6, s56, v248\n"
    "v_fma_f32 v7, v7, s56, v248\n"
    "v_fma_f32 v8, v8, s56, v248\n"
    "s_waitcnt lgkmcnt(0)\n"
    "LEB_%=:\n"
    "s_waitcnt vmcnt(6) lgkmcnt(0)\n"
    "s_barrier\n"
    "ds_read_b128 v[160:163], v240 offset:16384\n"
    "ds_read_b128 v[164:167], v241 offset:16384\n"
    "ds_read_b128 v[168:171], v242 offset:16384\n"
    "ds_read_b128 v[172:175], v243 offset:16384\n"
    "ds_read_b128 v[176:179], v244 offset:16384\n"
    "ds_read_b128 v[180:183], v245 offset:16384\n"
    "ds_read_b128 v[184:187], v246 offset:16384\n"
    "s_waitcnt lgkmcnt(6)\n"
    "v_mfma_f32_32x32x16_bf16 v[32:47], v[160:163], v[128:131], 0\n"
    "s_memtime s[84:85]\n"
    "s_memrealtime s[86:87]\n"
    "s_nop 15\n"
    "s_nop 7\n"
    "ds_read_b128 v[188:191], v247 offset:16384\n"
    "v_fma_f32 v9, v9, s56, v248\n"
    "v_fma_f32 v10, v10, s56, v248\n"
    "v_fma_f32 v11, v11, s56, v248\n"
    "v_fma_f32 v12, v12, s56, v248\n"
    "s_waitcnt lgkmcnt(6)\n"
    "v_mfma_f32_32x32x16_bf16 v[32:47], v[164:167], v[132:135], v[32:47]\n"
    "s_nop 15\n"
    "s_nop 7\n"
    "ds_read_b128 v[160:163], v240 offset:24576\n"
    "v_fma_f32 v13, v13, s56, v248\n"
    "v_fma_f32 v14, v14, s56, v248\n"
    "v_fma_f32 v15, v15, s56, v248\n"
    "v_mov_b32 v207, 0\n"
    "buffer_load_dword v196, v207, s[36:39], s53 offen\n"
    "buffer_load_dword v197, v207, s[40:43], s53 offen\n"
    "buffer_load_dword v198, v207, s[44:47], s53 offen\n"
    "buffer_load_dword v199, v207, s[48:51], s60 offen\n"
    "buffer_load_dword v200, v207, s[48:51], s61 offen\n"
    "s_waitcnt lgkmcnt(6)\n"
    "v_mfma_f32_32x32x16_bf16 v[32:47], v[168:171], v[136:139], v[32:47]\n"
    "s_nop 15\n"
    "s_nop 7\n"
    "ds_read_b128 v[164:167], v241 offset:24576\n"
    "v_fma_f32 v16, v16, s56, v248\n"
    "v_fma_f32 v17, v17, s56, v248\n"
    "s_add_u32 m0, s54, 16384\n"
    "s_add_u32 s55, s52, 16384\n"
    "buffer_load_dwordx4 v255, s[44:47], s55 offen nt lds\n"
    "v_fma_f32 v18, v18, s56, v248\n"
    "v_fma_f32 v19, v19, s56, v248\n"
    "s_waitcnt lgkmcnt(6)\n"
    "v_mfma_f32_32x32x16_bf16 v[32:47], v[172:175], v[140:143], v[32:47]\n"
    "s_cbranch_vccz LRE1_%=\n"
    "s_mov_b32 s91, s89\n"
    "LRL1_%=:\n"
    "s_sub_u32 s91, s91, 1\n"
    "s_cmp_ge_i32 s91, 0\n"
    "s_cbranch_scc1 LRL1_%=\n"
    "LRE1_%=:\n"
    "s_nop 15\n"
    "s_nop 7\n"
    "ds_read_b128 v[168:171], v242 offset:24576\n"
    "v_fma_f32 v20, v20, s56, v248\n"
    "v_fma_f32 v21, v21, s56, v248\n"
    "v_fma_f32 v22, v22, s56, v248\n"
    "v_fma_f32 v23, v23, s56, v248\n"
    "s_waitcnt lgkmcnt(6)\n"
    "v_mfma_f32_32x32x16_bf16 v[32:47], v[176:179], v[144:147], v[32:47]\n"
    "s_sub_u32 s88, s84, s90\n"
    "s_sub_u32 s85, s86, s92\n"
    "s_mov_b32 s90, s84\n"
    "s_mov_b32 s92, s86\n"
    "s_mul_i32 s87, s85, 205\n"
    "s_mul_i32 s91, s88, 10\n"
    "s_cmp_ge_u32 s91, s87\n"
    "s_cselect_b32 s88, s88, 0\n"
    "s_mul_i32 s88, s88, 64\n"
    "s_mov_b32 s89, 0\n"
    "s_mul_i32 s87, s85, 1407\n"
    "s_cmp_ge_u32 s88, s87\n"
    "s_addc_u32 s89, s89, 0\n"
    "s_mul_i32 s87, s85, 1472\n"
    "s_cmp_ge_u32 s88, s87\n"
    "s_addc_u32 s89, s89, 0\n"
    "s_mul_i32 s87, s85, 1537\n"
    "s_cmp_ge_u32 s88, s87\n"
    "s_addc_u32 s89, s89, 0\n"
    "s_mul_i32 s87, s85, 1602\n"
    "s_cmp_ge_u32 s88, s87\n"
    "s_addc_u32 s89, s89, 0\n"
    "s_mul_i32 s87, s85, 1667\n"
    "s_cmp_ge_u32 s88, s87\n"
    "s_addc_u32 s89, s89, 0\n"
    "s_mul_i32 s87, s85, 1732\n"
    "s_cmp_ge_u32 s88, s87\n"
    "s_addc_u32 s89, s89, 0\n"
    "s_cmp_lg_u32 s89, 0\n"
    "s_cselect_b64 vcc, -1, 0\n"
    "s_nop 15\n"
    "s_nop 7\n"
    "ds_read_b128 v[172:175], v243 offset:24576\n"
    "v_fma_f32 v24, v24, s56, v248\n"
    "v_fma_f32 v25, v25, s56, v248\n"
    "v_fma_f32 v26, v26, s56, v248\n"
    "v_fma_f32 v27, v27, s56, v248\n"
    "s_waitcnt lgkmcnt(6)\n"
    "v_mfma_f32_32x32x16_bf16 v[32:47], v[180:183], v[148:151], v[32:47]\n"
    "s_nop 15\n"
    "s_nop 7\n"
    "ds_read_b128 v[176:179], v244 offset:24576\n"
    "v_fma_f32 v28, v28, s56, v248\n"
    "v_fma_f32 v29, v29, s56, v248\n"
    "v_fma_f32 v30, v30, s56, v248\n"
    "v_fma_f32 v31, v31, s56, v248\n"
    "s_waitcnt lgkmcnt(6)\n"
    "v_mfma_f32_32x32x16_bf16 v[32:47], v[184:187], v[152:155], v[32:47]\n"
    "s_nop 15\n"
    "s_nop 7\n"
    "ds_read_b128 v[180:183], v245 offset:24576\n"
    "v_exp_f32 v234, v0\n"
    "v_exp_f32 v235, v1\n"
    "s_waitcnt lgkmcnt(6)\n"
    "v_mfma_f32_32x32x16_bf16 v[32:47], v[188:191], v[156:159], v[32:47]\n"
    "s_cbranch_vccz LRE2_%=\n"
    "s_mov_b32 s91, s89\n"
    "LRL2_%=:\n"
    "s_sub_u32 s91, s91, 1\n"
    "s_cmp_ge_i32 s91, 0\n"
    "s_cbranch_scc1 LRL2_%=\n"
    "LRE2_%=:\n"
    "s_nop 15\n"
    "s_nop 7\n"
    "ds_read_b128 v[184:187], v246 offset:24576\n"
    "v_cvt_pk_bf16_f32 v236, v234, v235\n"
    "v_mov_b32 v250, v234\n"
    "v_mov_b32 v251, v235\n"
    "s_waitcnt lgkmcnt(6)\n"
    "v_mfma_f32_32x32x16_bf16 v[48:63], v[160:163], v[128:131], 0\n"
    "s_nop 15\n"
    "s_nop 7\n"
    "ds_read_b128 v[188:191], v247 offset:24576\n"
    "v_exp_f32 v234, v2\n"
    "v_exp_f32 v235, v3\n"
    "s_waitcnt lgkmcnt(6)\n"
    "v_mfma_f32_32x32x16_bf16 v[48:63], v[164:167], v[132:135], v[48:63]\n"
    "s_nop 15\n"
    "s_nop 7\n"
    "buffer_load_dwordx4 v255, s[44:47], s55 offen offset:1024 nt lds\n"
    "s_waitcnt vmcnt(11)\n"
    "s_barrier\n"
    "ds_read_b128 v[160:163], v240 offset:32768\n"
    "v_cvt_pk_bf16_f32 v237, v234, v235\n"
    "v_add_f32 v250, v250, v234\n"
    "v_add_f32 v251, v251, v235\n"
    "s_waitcnt lgkmcnt(6)\n"
    "v_mfma_f32_32x32x16_bf16 v[48:63], v[168:171], v[136:139], v[48:63]\n"
    "s_nop 15\n"
    "s_nop 7\n"
    "ds_read_b128 v[164:167], v241 offset:32768\n"
    "v_fma_f32 v32, v32, s56, v248\n"
    "v_fma_f32 v33, v33, s56, v248\n"
    "v_fma_f32 v34, v34, s56, v248\n"
    "v_fma_f32 v35, v35, s56, v248\n"
    "s_waitcnt lgkmcnt(6)\n"
    "v_mfma_f32_32x32x16_bf16 v[48:63], v[172:175], v[140:143], v[48:63]\n"
    "s_cbranch_vccz LRE3_%=\n"
    "s_mov_b32 s91, s89\n"
    "LRL3_%=:\n"
    "s_sub_u32 s91, s91, 1\n"
    "s_cmp_ge_i32 s91, 0\n"
    "s_cbranch_scc1 LRL3_%=\n"
    "LRE3_%=:\n"
    "s_nop 15\n"
    "s_nop 7\n"
    "ds_read_b128 v[168:171], v242 offset:32768\n"
    "v_fma_f32 v36, v36, s56, v248\n"
    "v_fma_f32 v37, v37, s56, v248\n"
    "s_add_u32 m0, s54, 32768\n"
    "s_add_u32 s55, s52, 32768\n"
    "buffer_load_dwordx4 v255, s[44:47], s55 offen nt lds\n"
    "v_fma_f32 v38, v38, s56, v248\n"
    "v_fma_f32 v39, v39, s56, v248\n"
    "s_waitcnt lgkmcnt(6)\n"
    "v_mfma_f32_32x32x16_bf16 v[48:63], v[176:179], v[144:147], v[48:63]\n"
    "s_memtime s[84:85]\n"
    "s_memrealtime s[86:87]\n"
    "s_nop 15\n"
    "s_nop 7\n"
    "ds_read_b128 v[172:175], v243 offset:32768\n"
    "v_fma_f32 v40, v40, s56, v248\n"
    "v_fma_f32 v41, v41, s56, v248\n"
    "v_fma_f32 v42, v42, s56, v248\n"
    "v_fma_f32 v43, v43, s56, v248\n"
    "s_waitcnt lgkmcnt(6)\n"
    "v_mfma_f32_32x32x16_bf16 v[48:63], v[180:183], v[148:151], v[48:63]\n"
    "s_nop 15\n"
    "s_nop 7\n"
    "ds_read_b128 v[176:179], v244 offset:32768\n"
    "v_fma_f32 v44, v44, s56, v248\n"
    "v_fma_f32 v45, v45, s56, v248\n"
    "v_fma_f32 v46, v46, s56, v248\n"
    "v_fma_f32 v47, v47, s56, v248\n"
    "s_waitcnt lgkmcnt(6)\n"
    "v_mfma_f32_32x32x16_bf16 v[48:63], v[184:187], v[152:155], v[48:63]\n"
    "s_nop 15\n"
    "s_nop 7\n"
    "ds_read_b128 v[180:183], v245 offset:32768\n"
    "v_exp_f32 v234, v4\n"
    "v_exp_f32 v235, v5\n"
    "s_waitcnt lgkmcnt(6)\n"
    "v_mfma_f32_32x32x16_bf16 v[48:63], v[188:191], v[156:159], v[48:63]\n"
    "s_cbranch_vccz LRE4_%=\n"
    "s_mov_b32 s91, s89\n"
    "LRL4_%=:\n"
    "s_sub_u32 s91, s91, 1\n"
    "s_cmp_ge_i32 s91, 0\n"
    "s_cbranch_scc1 LRL4_%=\n"
    "LRE4_%=:\n"
    "s_nop 15\n"
    "s_nop 7\n"
    "ds_read_b128 v[184:187], v246 offset:32768\n"
    "v_cvt_pk_bf16_f32 v238, v234, v235\n"
    "v_add_f32 v250, v250, v234\n"
    "v_add_f32 v251, v251, v235\n"
    "s_waitcnt lgkmcnt(6)\n"
    "v_mfma_f32_32x32x16_bf16 v[64:79], v[160:163], v[128:131], 0\n"
    "s_sub_u32 s88, s84, s90\n"
    "s_sub_u32 s85, s86, s92\n"
    "s_mov_b32 s90, s84\n"
    "s_mov_b32 s92, s86\n"
    "s_mul_i32 s87, s85, 205\n"
    "s_mul_i32 s91, s88, 10\n"
    "s_cmp_ge_u32 s91, s87\n"
    "s_cselect_b32 s88, s88, 0\n"
    "s_mul_i32 s88, s88, 52\n"
    "s_mov_b32 s89, 0\n"
    "s_mul_i32 s87, s85, 1087\n"
    "s_cmp_ge_u32 s88, s87\n"
    "s_addc_u32 s89, s89, 0\n"
    "s_mul_i32 s87, s85, 1152\n"
    "s_cmp_ge_u32 s88, s87\n"
    "s_addc_u32 s89, s89, 0\n"
    "s_mul_i32 s87, s85, 1217\n"
    "s_cmp_ge_u32 s88, s87\n"
    "s_addc_u32 s89, s89, 0\n"
    "s_mul_i32 s87, s85, 1282\n"
    "s_cmp_ge_u32 s88, s87\n"
    "s_addc_u32 s89, s89, 0\n"
    "s_mul_i32 s87, s85, 1347\n"
    "s_cmp_ge_u32 s88, s87\n"
    "s_addc_u32 s89, s89, 0\n"
    "s_mul_i32 s87, s85, 1412\n"
    "s_cmp_ge_u32 s88, s87\n"
    "s_addc_u32 s89, s89, 0\n"
    "s_cmp_lg_u32 s89, 0\n"
    "s_cselect_b64 vcc, -1, 0\n"
    "s_nop 15\n"
    "ds_read_b128 v[188:191], v247 offset:32768\n"
    "v_exp_f32 v234, v6\n"
    "v_exp_f32 v235, v7\n"
    "s_waitcnt lgkmcnt(6)\n"
    "v_mfma_f32_32x32x16_bf16 v[64:79], v[164:167], v[132:135], v[64:79]\n"
    "s_nop 15\n"
    "ds_read_b128 v[160:163], v240 offset:40960\n"
    "v_cvt_pk_bf16_f32 v239, v234, v235\n"
    "v_add_f32 v250, v250, v234\n"
    "v_add_f32 v251, v251, v235\n"
    "s_waitcnt lgkmcnt(6)\n"
    "v_mfma_f32_32x32x16_bf16 v[64:79], v[168:171], v[136:139], v[64:79]\n"
    "s_nop 15\n"
    "ds_read_b128 v[164:167], v241 offset:40960\n"
    "v_fma_f32 v48, v48, s56, v248\n"
    "v_fma_f32 v49, v49, s56, v248\n"
    "buffer_load_dwordx4 v255, s[44:47], s55 offen offset:1024 nt lds\n"
    "v_fma_f32 v50, v50, s56, v248\n"
    "v_fma_f32 v51, v51, s56, v248\n"
    "s_waitcnt lgkmcnt(6)\n"
    "v_mfma_f32_32x32x16_bf16 v[64:79], v[172:175], v[140:143], v[64:79]\n"
    "s_cbranch_vccz LRE5_%=\n"
    "s_mov_b32 s91, s89\n"
    "LRL5_%=:\n"
    "s_sub_u32 s91, s91, 1\n"
    "s_cmp_ge_i32 s91, 0\n"
    "s_cbranch_scc1 LRL5_%=\n"
    "LRE5_%=:\n"
    "s_nop 15\n"
    "ds_read_b128 v[168:171], v242 offset:40960\n"
    "v_fma_f32 v52, v52, s56, v248\n"
    "v_fma_f32 v53, v53, s56, v248\n"
    "v_fma_f32 v54, v54, s56, v248\n"
    "v_fma_f32 v55, v55, s56, v248\n"
    "s_waitcnt lgkmcnt(6)\n"
    "v_mfma_f32_32x32x16_bf16 v[64:79], v[176:179], v[144:147], v[64:79]\n"
    "s_nop 15\n"
    "ds_read_b128 v[172:175], v243 offset:40960\n"
    "v_fma_f32 v56, v56, s56, v248\n"
    "v_fma_f32 v57, v57, s56, v248\n"
    "v_fma_f32 v58, v58, s56, v248\n"
    "v_fma_f32 v59, v59, s56, v248\n"
    "s_waitcnt lgkmcnt(6)\n"
    "v_mfma_f32_32x32x16_bf16 v[64:79], v[180:183], v[148:151], v[64:79]\n"
    "s_nop 15\n"
    "ds_read_b128 v[176:179], v244 offset:40960\n"
    "v_fma_f32 v60, v60, s56, v248\n"
    "v_fma_f32 v61, v61, s56, v248\n"
    "v_fma_f32 v62, v62, s56, v248\n"
    "v_fma_f32 v63, v63, s56, v248\n"
    "s_waitcnt lgkmcnt(6)\n"
    "v_mfma_f32_32x32x16_bf16 v[64:79], v[184:187], v[152:155], v[64:79]\n"
    "s_nop 15\n"
    "ds_read_b128 v[180:183], v245 offset:40960\n"
    "v_exp_f32 v234, v8\n"
    "v_exp_f32 v235, v9\n"
    "s_waitcnt lgkmcnt(6)\n"
    "v_mfma_f32_32x32x16_bf16 v[64:79], v[188:191], v[156:159], v[64:79]\n"
    "s_cbranch_vccz LRE6_%=\n"
    "s_mov_b32 s91, s89\n"
    "LRL6_%=:\n"
    "s_sub_u32 s91, s91, 1\n"
    "s_cmp_ge_i32 s91, 0\n"
    "s_cbranch_scc1 LRL6_%=\n"
    "LRE6_%=:\n"
    "s_nop 15\n"
    "ds_read_b128 v[184:187], v246 offset:40960\n"
    "s_waitcnt lgkmcnt(6)\n"
    "v_mfma_f32_32x32x16_bf16 v[80:95], v[160:163], v[128:131], 0\n"
    "s_nop 15\n"
    "ds_read_b128 v[188:191], v247 offset:40960\n"
    "s_waitcnt lgkmcnt(6)\n"
    "v_mfma_f32_32x32x16_bf16 v[80:95], v[164:167], v[132:135], v[80:95]\n"
    "s_nop 15\n"
    "s_waitcnt vmcnt(11)\n"
    "s_barrier\n"
    "ds_read_b128 v[160:163], v240 offset:49152\n"
    "s_waitcnt lgkmcnt(6)\n"
    "v_mfma_f32_32x32x16_bf16 v[80:95], v[168:171], v[136:139], v[80:95]\n"
    "s_nop 15\n"
    "ds_read_b128 v[164:167], v241 offset:49152\n"
    "v_fma_f32 v64, v64, s56, v248\n"
    "v_fma_f32 v65, v65, s56, v248\n"
    "v_fma_f32 v66, v66, s56, v248\n"
    "v_fma_f32 v67, v67, s56, v248\n"
    "s_waitcnt lgkmcnt(6)\n"
    "v_mfma_f32_32x32x16_bf16 v[80:95], v[172:175], v[140:143], v[80:95]\n"
    "s_cbranch_vccz LRE7_%=\n"
    "s_mov_b32 s91, s89\n"
    "LRL7_%=:\n"
    "s_sub_u32 s91, s91, 1\n"
    "s_cmp_ge_i32 s91, 0\n"
    "s_cbranch_scc1 LRL7_%=\n"
    "LRE7_%=:\n"
    "s_nop 15\n"
    "ds_read_b128 v[168:171], v242 offset:49152\n"
    "v_fma_f32 v68, v68, s56, v248\n"
    "v_fma_f32 v69, v69, s56, v248\n"
    "s_add_u32 m0, s54, 49152\n"
    "s_add_u32 s55, s52, 49152\n"
    "buffer_load_dwordx4 v255, s[44:47], s55 offen nt lds\n"
    "v_fma_f32 v70, v70, s56, v248\n"
    "v_fma_f32 v71, v71, s56, v248\n"
    "s_waitcnt lgkmcnt(6)\n"
    "v_mfma_f32_32x32x16_bf16 v[80:95], v[176:179], v[144:147], v[80:95]\n"
    "s_memtime s[84:85]\n"
    "s_memrealtime s[86:87]\n"
    "s_nop 15\n"
    "ds_read_b128 v[172:175], v243 offset:49152\n"
    "v_fma_f32 v72, v72, s56, v248\n"
    "v_fma_f32 v73, v73, s56, v248\n"
    "v_fma_f32 v74, v74, s56, v248\n"
    "v_fma_f32 v75, v75, s56, v248\n"
    "s_waitcnt lgkmcnt(6)\n"
    "v_mfma_f32_32x32x16_bf16 v[80:95], v[180:183], v[148:151], v[80:95]\n"
    "s_nop 15\n"
    "ds_read_b128 v[176:179], v244 offset:49152\n"
    "v_fma_f32 v76, v76, s56, v248\n"
    "v_fma_f32 v77, v77, s56, v248\n"
    "v_fma_f32 v78, v78, s56, v248\n"
    "v_fma_f32 v79, v79, s56, v248\n"
    "s_waitcnt lgkmcnt(6)\n"
    "v_mfma_f32_32x32x16_bf16 v[80:95], v[184:187], v[152:155], v[80:95]\n"
    "s_nop 15\n"
    "ds_read_b128 v[180:183], v245 offset:49152\n"
    "v_xor_b32 v252, 0x10000, v252\n"
    "s_waitcnt lgkmcnt(6)\n"
    "v_mfma_f32_32x32x16_bf16 v[80:95], v[188:191], v[156:159], v[80:95]\n"
    "s_cbranch_vccz LRE8_%=\n"
    "s_mov_b32 s91, s89\n"
    "LRL8_%=:\n"
    "s_sub_u32 s91, s91, 1\n"
    "s_cmp_ge_i32 s91, 0\n"
    "s_cbranch_scc1 LRL8_%=\n"
    "LRE8_%=:\n"
    "s_nop 15\n"
    "ds_read_b128 v[184:187], v246 offset:49152\n"
    "s_waitcnt lgkmcnt(6)\n"
    "v_mfma_f32_32x32x16_bf16 v[96:111], v[160:163], v[128:131], 0\n"
    "s_sub_u32 s88, s84, s90\n"
    "s_sub_u32 s85, s86, s92\n"
    "s_mov_b32 s90, s84\n"
    "s_mov_b32 s92, s86\n"
    "s_mul_i32 s87, s85, 205\n"
    "s_mul_i32 s91, s88, 10\n"
    "s_cmp_ge_u32 s91, s87\n"
    "s_cselect_b32 s88, s88, 0\n"
    "s_mul_i32 s88, s88, 47\n"
    "s_mov_b32 s89, 0\n"
    "s_mul_i32 s87, s85, 767\n"
    "s_cmp_ge_u32 s88, s87\n"
    "s_addc_u32 s89, s89, 0\n"
    "s_mul_i32 s87, s85, 832\n"
    "s_cmp_ge_u32 s88, s87\n"
    "s_addc_u32 s89, s89, 0\n"
    "s_mul_i32 s87, s85, 897\n"
    "s_cmp_ge_u32 s88, s87\n"
    "s_addc_u32 s89, s89, 0\n"
    "s_mul_i32 s87, s85, 962\n"
    "s_cmp_ge_u32 s88, s87\n"
    "s_addc_u32 s89, s89, 0\n"
    "s_mul_i32 s87, s85, 1027\n"
    "s_cmp_ge_u32 s88, s87\n"
    "s_addc_u32 s89, s89, 0\n"
    "s_mul_i32 s87, s85, 1092\n"
    "s_cmp_ge_u32 s88, s87\n"
    "s_addc_u32 s89, s89, 0\n"
    "s_cmp_lg_u32 s89, 0\n"
    "s_cselect_b64 vcc, -1, 0\n"
    "s_nop 7\n"
    "ds_read_b128 v[188:191], v247 offset:49152\n"
    "s_waitcnt lgkmcnt(6)\n"
    "v_mfma_f32_32x32x16_bf16 v[96:111], v[164:167], v[132:135], v[96:111]\n"
    "s_nop 7\n"
    "ds_read_b128 v[160:163], v240 offset:57344\n"
    "s_waitcnt lgkmcnt(6)\n"
    "v_mfma_f32_32x32x16_bf16 v[96:111], v[168:171], v[136:139], v[96:111]\n"
    "s_nop 7\n"
    "ds_read_b128 v[164:167], v241 offset:57344\n"
    "v_fma_f32 v80, v80, s56, v248\n"
    "v_fma_f32 v81, v81, s56, v248\n"
    "buffer_load_dwordx4 v255, s[44:47], s55 offen offset:1024 nt lds\n"
    "v_fma_f32 v82, v82, s56, v248\n"
    "v_fma_f32 v83, v83, s56, v248\n"
    "s_waitcnt lgkmcnt(6)\n"
    "v_mfma_f32_32x32x16_bf16 v[96:111], v[172:175], v[140:143], v[96:111]\n"
    "s_cbranch_vccz LRE9_%=\n"
    "s_mov_b32 s91, s89\n"
    "LRL9_%=:\n"
    "s_sub_u32 s91, s91, 1\n"
    "s_cmp_ge_i32 s91, 0\n"
    "s_cbranch_scc1 LRL9_%=\n"
    "LRE9_%=:\n"
    "s_nop 7\n"
    "ds_read_b128 v[168:171], v242 offset:57344\n"
    "v_fma_f32 v84, v84, s56, v248\n"
    "v_fma_f32 v85, v85, s56, v248\n"
    "v_fma_f32 v86, v86, s56, v248\n"
    "v_fma_f32 v87, v87, s56, v248\n"
    "s_waitcnt lgkmcnt(6)\n"
    "v_mfma_f32_32x32x16_bf16 v[96:111], v[176:179], v[144:147], v[96:111]\n"
    "s_nop 7\n"
    "ds_read_b128 v[172:175], v243 offset:57344\n"
    "v_fma_f32 v88, v88, s56, v248\n"
    "v_fma_f32 v89, v89, s56, v248\n"
    "v_fma_f32 v90, v90, s56, v248\n"
    "v_fma_f32 v91, v91, s56, v248\n"
    "s_waitcnt lgkmcnt(6)\n"
    "v_mfma_f32_32x32x16_bf16 v[96:111], v[180:183], v[148:151], v[96:111]\n"
    "s_nop 7\n"
    "ds_read_b128 v[176:179], v244 offset:57344\n"
    "v_fma_f32 v92, v92, s56, v248\n"
    "v_fma_f32 v93, v93, s56, v248\n"
    "v_fma_f32 v94, v94, s56, v248\n"
    "v_fma_f32 v95, v95, s56, v248\n"
    "s_waitcnt lgkmcnt(6)\n"
    "v_mfma_f32_32x32x16_bf16 v[96:111], v[184:187], v[152:155], v[96:111]\n"
    "s_nop 7\n"
    "ds_read_b128 v[180:183], v245 offset:57344\n"
    "s_waitcnt lgkmcnt(6)\n"
    "v_mfma_f32_32x32x16_bf16 v[96:111], v[188:191], v[156:159], v[96:111]\n"
    "s_cbranch_vccz LRE10_%=\n"
    "s_mov_b32 s91, s89\n"
    "LRL10_%=:\n"
    "s_sub_u32 s91, s91, 1\n"
    "s_cmp_ge_i32 s91, 0\n"
    "s_cbranch_scc1 LRL10_%=\n"
    "LRE10_%=:\n"
    "s_nop 7\n"
    "ds_read_b128 v[184:187], v246 offset:57344\n"
    "s_waitcnt lgkmcnt(6)\n"
    "v_mfma_f32_32x32x16_bf16 v[112:127], v[160:163], v[128:131], 0\n"
    "s_nop 7\n"
    "ds_read_b128 v[188:191], v247 offset:57344\n"
    "s_waitcnt lgkmcnt(6)\n"
    "v_mfma_f32_32x32x16_bf16 v[112:127], v[164:167], v[132:135], v[112:127]\n"
    "s_nop 7\n"
    "s_waitcnt lgkmcnt(5)\n"
    "v_mfma_f32_32x32x16_bf16 v[112:127], v[168:171], v[136:139], v[112:127]\n"
    "s_nop 7\n"
    "v_fma_f32 v96, v96, s56, v248\n"
    "v_fma_f32 v97, v97, s56, v248\n"
    "v_fma_f32 v98, v98, s56, v248\n"
    "v_fma_f32 v99, v99, s56, v248\n"
    "s_waitcnt lgkmcnt(4)\n"
    "v_mfma_f32_32x32x16_bf16 v[112:127], v[172:175], v[140:143], v[112:127]\n"
    "s_cbranch_vccz LRE11_%=\n"
    "s_mov_b32 s91, s89\n"
    "LRL11_%=:\n"
    "s_sub_u32 s91, s91, 1\n"
    "s_cmp_ge_i32 s91, 0\n"
    "s_cbranch_scc1 LRL11_%=\n"
    "LRE11_%=:\n"
    "s_nop 7\n"
    "v_fma_f32 v100, v100, s56, v248\n"
    "v_fma_f32 v101, v101, s56, v248\n"
    "v_fma_f32 v102, v102, s56, v248\n"
    "v_fma_f32 v103, v103, s56, v248\n"
    "s_waitcnt lgkmcnt(3)\n"
    "v_mfma_f32_32x32x16_bf16 v[112:127], v[176:179], v[144:147], v[112:127]\n"
    "s_memtime s[84:85]\n"
    "s_memrealtime s[86:87]\n"
    "s_nop 7\n"
    "v_fma_f32 v104, v104, s56, v248\n"
    "v_fma_f32 v105, v105, s56, v248\n"
    "v_fma_f32 v106, v106, s56, v248\n"
    "v_fma_f32 v107, v107, s56, v248\n"
    "s_waitcnt lgkmcnt(2)\n"
    "v_mfma_f32_32x32x16_bf16 v[112:127], v[180:183], v[148:151], v[112:127]\n"
    "s_nop 7\n"
    "v_fma_f32 v108, v108, s56, v248\n"
    "v_fma_f32 v109, v109, s56, v248\n"
    "v_fma_f32 v110, v110, s56, v248\n"
    "v_fma_f32 v111, v111, s56, v248\n"
    "s_waitcnt lgkmcnt(1)\n"
    "v_mfma_f32_32x32x16_bf16 v[112:127], v[184:187], v[152:155], v[112:127]\n"
    "s_nop 7\n"
    "s_waitcnt lgkmcnt(0)\n"
    "v_mfma_f32_32x32x16_bf16 v[112:127], v[188:191], v[156:159], v[112:127]\n"
    "s_cbranch_vccz LRE12_%=\n"
    "s_mov_b32 s91, s89\n"
    "LRL12_%=:\n"
    "s_sub_u32 s91, s91, 1\n"
    "s_cmp_ge_i32 s91, 0\n"
    "s_cbranch_scc1 LRL12_%=\n"
    "LRE12_%=:\n"
    "s_nop 7\n"
    "v_xor_b32 v244, 0, v253\n"
    "v_xor_b32 v245, 64, v253\n"
    "v_xor_b32 v246, 128, v253\n"
    "v_xor_b32 v247, 192, v253\n"
    "s_waitcnt vmcnt(11)\n"
    "s_barrier\n"
    "ds_read_b64_tr_b16 v[208:209], v244 offset:0\n"
    "ds_read_b64_tr_b16 v[210:211], v244 offset:2048\n"
    "ds_read_b64_tr_b16 v[212:213], v245 offset:0\n"
    "ds_read_b64_tr_b16 v[214:215], v245 offset:2048\n"
    "ds_read_b64_tr_b16 v[216:217], v246 offset:0\n"
    "ds_read_b64_tr_b16 v[218:219], v246 offset:2048\n"
    "ds_read_b64_tr_b16 v[220:221], v247 offset:0\n"
    "ds_read_b64_tr_b16 v[222:223], v247 offset:2048\n"
    "ds_read_b64_tr_b16 v[224:225], v244 offset:4096\n"
    "ds_read_b64_tr_b16 v[226:227], v244 offset:6144\n"
    "ds_read_b64_tr_b16 v[228:229], v245 offset:4096\n"
    "ds_read_b64_tr_b16 v[230:231], v245 offset:6144\n"
    "s_branch LNX1_%=\n"
    "LND16_%=:\n"
    "s_mov_b32 s91, s89\n"
    "LNL16_%=:\n"
    "s_sub_u32 s91, s91, 1\n"
    "s_cmp_gt_i32 s91, 0\n"
    "s_cbranch_scc1 LNL16_%=\n"
    "s_branch LNR16_%=\n"
    "LND17_%=:\n"
    "s_mov_b32 s91, s89\n"
    "LNL17_%=:\n"
    "s_sub_u32 s91, s91, 1\n"
    "s_cmp_gt_i32 s91, 0\n"
    "s_cbranch_scc1 LNL17_%=\n"
    "s_branch LNR17_%=\n"
    "LND18_%=:\n"
    "s_mov_b32 s91, s89\n"
    "LNL18_%=:\n"
    "s_sub_u32 s91, s91, 1\n"
    "s_cmp_gt_i32 s91, 0\n"
    "s_cbranch_scc1 LNL18_%=\n"
    "s_branch LNR18_%=\n"
    "LND19_%=:\n"
    "s_mov_b32 s91, s89\n"
    "LNL19_%=:\n"
    "s_sub_u32 s91, s91, 1\n"
    "s_cmp_gt_i32 s91, 0\n"
    "s_cbranch_scc1 LNL19_%=\n"
    "s_branch LNR19_%=\n"
    "LNX1_%=:\n"
    "s_waitcnt lgkmcnt(10)\n"
    "v_mfma_f32_32x32x16_bf16 v[128:143], v[208:211], v[236:239], 0\n"
    "s_sub_u32 s88, s84, s90\n"
    "s_sub_u32 s85, s86, s92\n"
    "s_mov_b32 s90, s84\n"
    "s_mov_b32 s92, s86\n"
    "s_mul_i32 s87, s85, 182\n"
    "s_mul_i32 s91, s88, 10\n"
    "s_cmp_ge_u32 s91, s87\n"
    "s_cselect_b32 s88, s88, 0\n"
    "s_mul_i32 s88, s88, 48\n"
    "s_mov_b32 s89, 0\n"
    "s_mul_i32 s87, s85, 847\n"
    "s_cmp_ge_u32 s88, s87\n"
    "s_addc_u32 s89, s89, 0\n"
    "s_mul_i32 s87, s85, 912\n"
    "s_cmp_ge_u32 s88, s87\n"
    "s_addc_u32 s89, s89, 0\n"
    "s_mul_i32 s87, s85, 977\n"
    "s_cmp_ge_u32 s88, s87\n"
    "s_addc_u32 s89, s89, 0\n"
    "s_mul_i32 s87, s85, 1042\n"
    "s_cmp_ge_u32 s88, s87\n"
    "s_addc_u32 s89, s89, 0\n"
    "s_mul_i32 s87, s85, 1107\n"
    "s_cmp_ge_u32 s88, s87\n"
    "s_addc_u32 s89, s89, 0\n"
    "s_mul_i32 s87, s85, 1172\n"
    "s_cmp_ge_u32 s88, s87\n"
    "s_addc_u32 s89, s89, 0\n"
    "s_cmp_lg_u32 s89, 0\n"
    "s_cselect_b64 vcc, -1, 0\n"
    "v_cvt_pk_bf16_f32 v240, v234, v235\n"
    "v_add_f32 v250, v250, v234\n"
    "v_add_f32 v251, v251, v235\n"
    "ds_read_b64_tr_b16 v[232:233], v246 offset:4096\n"
    "ds_read_b64_tr_b16 v[234:235], v246 offset:6144\n"
    "v_exp_f32 v10, v10\n"
    "v_exp_f32 v11, v11\n"
    "s_waitcnt lgkmcnt(10)\n"
    "v_mfma_f32_32x32x16_bf16 v[144:159], v[212:215], v[236:239], 0\n"
    "ds_read_b64_tr_b16 v[208:209], v247 offset:4096\n"
    "ds_read_b64_tr_b16 v[210:211], v247 offset:6144\n"
    "v_exp_f32 v12, v12\n"
    "v_exp_f32 v13, v13\n"
    "v_cvt_pk_bf16_f32 v241, v10, v11\n"
    "v_add_f32 v250, v250, v10\n"
    "v_add_f32 v251, v251, v11\n"
    "s_waitcnt lgkmcnt(10)\n"
    "v_mfma_f32_32x32x16_bf16 v[160:175], v[216:219], v[236:239], 0\n"
    "ds_read_b64_tr_b16 v[212:213], v244 offset:8192\n"
    "ds_read_b64_tr_b16 v[214:215], v244 offset:10240\n"
    "v_exp_f32 v14, v14\n"
    "v_exp_f32 v15, v15\n"
    "v_cvt_pk_bf16_f32 v242, v12, v13\n"
    "v_add_f32 v250, v250, v12\n"
    "v_add_f32 v251, v251, v13\n"
    "s_add_u32 m0, s54, 65536\n"
    "s_add_u32 s55, s53, 0\n"
    "buffer_load_dwordx4 v254, s[36:39], s55 offen nt lds\n"
    "v_fma_f32 v112, v112, s56, v248\n"
    "s_waitcnt lgkmcnt(10)\n"
    "v_mfma_f32_32x32x16_bf16 v[176:191], v[220:223], v[236:239], 0\n"
    "s_cbranch_vccnz LND16_%=\n"
    "LNR16_%=:\n"
    "ds_read_b64_tr_b16 v[216:217], v245 offset:8192\n"
    "ds_read_b64_tr_b16 v[218:219], v245 offset:10240\n"
    "v_exp_f32 v16, v16\n"
    "v_exp_f32 v17, v17\n"
    "v_cvt_pk_bf16_f32 v243, v14, v15\n"
    "v_add_f32 v250, v250, v14\n"
    "v_add_f32 v251, v251, v15\n"
    "v_fma_f32 v113, v113, s56, v248\n"
    "s_waitcnt lgkmcnt(10)\n"
    "v_mfma_f32_32x32x16_bf16 v[128:143], v[224:227], v[240:243], v[128:143]\n"
    "ds_read_b64_tr_b16 v[220:221], v246 offset:8192\n"
    "ds_read_b64_tr_b16 v[222:223], v246 offset:10240\n"
    "v_exp_f32 v18, v18\n"
    "v_exp_f32 v19, v19\n"
    "v_cvt_pk_bf16_f32 v236, v16, v17\n"
    "v_add_f32 v250, v250, v16\n"
    "v_add_f32 v251, v251, v17\n"
    "v_fma_f32 v114, v114, s56, v248\n"
    "s_waitcnt lgkmcnt(10)\n"
    "v_mfma_f32_32x32x16_bf16 v[144:159], v[228:231], v[240:243], v[144:159]\n"
    "ds_read_b64_tr_b16 v[224:225], v247 offset:8192\n"
    "ds_read_b64_tr_b16 v[226:227], v247 offset:10240\n"
    "v_exp_f32 v20, v20\n"
    "v_exp_f32 v21, v21\n"
    "v_cvt_pk_bf16_f32 v237, v18, v19\n"
    "v_add_f32 v250, v250, v18\n"
    "v_add_f32 v251, v251, v19\n"
    "buffer_load_dwordx4 v249, s[36:39], s55 offen offset:1024 nt lds\n"
    "v_fma_f32 v115, v115, s56, v248\n"
    "s_waitcnt lgkmcnt(10)\n"
    "v_mfma_f32_32x32x16_bf16 v[160:175], v[232:235], v[240:243], v[160:175]\n"
    "ds_read_b64_tr_b16 v[228:229], v244 offset:12288\n"
    "ds_read_b64_tr_b16 v[230:231], v244 offset:14336\n"
    "v_exp_f32 v22, v22\n"
    "v_exp_f32 v23, v23\n"
    "v_cvt_pk_bf16_f32 v238, v20, v21\n"
    "v_add_f32 v250, v250, v20\n"
    "v_add_f32 v251, v251, v21\n"
    "v_fma_f32 v116, v116, s56, v248\n"
    "s_waitcnt lgkmcnt(10)\n"
    "v_mfma_f32_32x32x16_bf16 v[176:191], v[208:211], v[240:243], v[176:191]\n"
    "s_cbranch_vccnz LND17_%=\n"
    "LNR17_%=:\n"
    "ds_read_b64_tr_b16 v[232:233], v245 offset:12288\n"
    "ds_read_b64_tr_b16 v[234:235], v245 offset:14336\n"
    "v_exp_f32 v24, v24\n"
    "v_exp_f32 v25, v25\n"
    "v_cvt_pk_bf16_f32 v239, v22, v23\n"
    "v_add_f32 v250, v250, v22\n"
    "v_add_f32 v251, v251, v23\n"
    "s_add_u32 m0, s54, 81920\n"
    "s_add_u32 s55, s53, 16384\n"
    "buffer_load_dwordx4 v254, s[36:39], s55 offen nt lds\n"
    "v_fma_f32 v117, v117, s56, v248\n"
    "s_waitcnt lgkmcnt(10)\n"
    "v_mfma_f32_32x32x16_bf16 v[128:143], v[212:215], v[236:239], v[128:143]\n"
    "ds_read_b64_tr_b16 v[208:209], v246 offset:12288\n"
    "ds_read_b64_tr_b16 v[210:211], v246 offset:14336\n"
    "v_exp_f32 v26, v26\n"
    "v_exp_f32 v27, v27\n"
    "v_cvt_pk_bf16_f32 v240, v24, v25\n"
    "v_add_f32 v250, v250, v24\n"
    "v_add_f32 v251, v251, v25\n"
    "v_fma_f32 v118, v118, s56, v248\n"
    "s_waitcnt lgkmcnt(10)\n"
    "v_mfma_f32_32x32x16_bf16 v[144:159], v[216:219], v[236:239], v[144:159]\n"
    "ds_read_b64_tr_b16 v[212:213], v247 offset:12288\n"
    "ds_read_b64_tr_b16 v[214:215], v247 offset:14336\n"
    "v_exp_f32 v28, v28\n"
    "v_exp_f32 v29, v29\n"
    "v_cvt_pk_bf16_f32 v241, v26, v27\n"
    "v_add_f32 v250, v250, v26\n"
    "v_add_f32 v251, v251, v27\n"
    "v_fma_f32 v119, v119, s56, v248\n"
    "s_waitcnt lgkmcnt(10)\n"
    "v_mfma_f32_32x32x16_bf16 v[160:175], v[220:223], v[236:239], v[160:175]\n"
    "buffer_load_dwordx4 v249, s[36:39], s55 offen offset:1024 nt lds\n"
    "s_waitcnt vmcnt(8)\n"
    "s_barrier\n"
    "ds_read_b64_tr_b16 v[216:217], v244 offset:16384\n"
    "ds_read_b64_tr_b16 v[218:219], v244 offset:18432\n"
    "v_exp_f32 v30, v30\n"
    "v_exp_f32 v31, v31\n"
    "v_cvt_pk_bf16_f32 v242, v28, v29\n"
    "v_add_f32 v250, v250, v28\n"
    "v_add_f32 v251, v251, v29\n"
    "v_fma_f32 v120, v120, s56, v248\n"
    "s_waitcnt lgkmcnt(10)\n"
    "v_mfma_f32_32x32x16_bf16 v[176:191], v[224:227], v[236:239], v[176:191]\n"
    "s_cbranch_vccnz LND18_%=\n"
    "LNR18_%=:\n"
    "ds_read_b64_tr_b16 v[220:221], v245 offset:16384\n"
    "ds_read_b64_tr_b16 v[222:223], v245 offset:18432\n"
    "v_exp_f32 v32, v32\n"
    "v_exp_f32 v33, v33\n"
    "v_cvt_pk_bf16_f32 v243, v30, v31\n"
    "v_add_f32 v250, v250, v30\n"
    "v_add_f32 v251, v251, v31\n"
    "v_fma_f32 v121, v121, s56, v248\n"
    "s_waitcnt lgkmcnt(10)\n"
    "v_mfma_f32_32x32x16_bf16 v[128:143], v[228:231], v[240:243], v[128:143]\n"
    "s_memtime s[84:85]\n"
    "s_memrealtime s[86:87]\n"
    "ds_read_b64_tr_b16 v[224:225], v246 offset:16384\n"
    "ds_read_b64_tr_b16 v[226:227], v246 offset:18432\n"
    "v_exp_f32 v34, v34\n"
    "v_exp_f32 v35, v35\n"
    "v_cvt_pk_bf16_f32 v236, v32, v33\n"
    "v_add_f32 v250, v250, v32\n"
    "v_add_f32 v251, v251, v33\n"
    "s_add_u32 m0, s54, 98304\n"
    "s_add_u32 s55, s53, 32768\n"
    "buffer_load_dwordx4 v254, s[36:39], s55 offen nt lds\n"
    "v_fma_f32 v122, v122, s56, v248\n"
    "s_waitcnt lgkmcnt(10)\n"
    "v_mfma_f32_32x32x16_bf16 v[144:159], v[232:235], v[240:243], v[144:159]\n"
    "ds_read_b64_tr_b16 v[228:229], v247 offset:16384\n"
    "ds_read_b64_tr_b16 v[230:231], v247 offset:18432\n"
    "v_exp_f32 v36, v36\n"
    "v_exp_f32 v37, v37\n"
    "v_cvt_pk_bf16_f32 v237, v34, v35\n"
    "v_add_f32 v250, v250, v34\n"
    "v_add_f32 v251, v251, v35\n"
    "v_fma_f32 v123, v123, s56, v248\n"
    "s_waitcnt lgkmcnt(10)\n"
    "v_mfma_f32_32x32x16_bf16 v[160:175], v[208:211], v[240:243], v[160:175]\n"
    "ds_read_b64_tr_b16 v[232:233], v244 offset:20480\n"
    "ds_read_b64_tr_b16 v[234:235], v244 offset:22528\n"
    "v_exp_f32 v38, v38\n"
    "v_exp_f32 v39, v39\n"
    "v_cvt_pk_bf16_f32 v238, v36, v37\n"
    "v_add_f32 v250, v250, v36\n"
    "v_add_f32 v251, v251, v37\n"
    "v_fma_f32 v124, v124, s56, v248\n"
    "s_waitcnt lgkmcnt(10)\n"
    "v_mfma_f32_32x32x16_bf16 v[176:191], v[212:215], v[240:243], v[176:191]\n"
    "s_cbranch_vccnz LND19_%=\n"
    "LNR19_%=:\n"
    "ds_read_b64_tr_b16 v[208:209], v245 offset:20480\n"
    "ds_read_b64_tr_b16 v[210:211], v245 offset:22528\n"
    "v_exp_f32 v40, v40\n"
    "v_exp_f32 v41, v41\n"
    "v_cvt_pk_bf16_f32 v239, v38, v39\n"
    "v_add_f32 v250, v250, v38\n"
    "v_add_f32 v251, v251, v39\n"
    "buffer_load_dwordx4 v249, s[36:39], s55 offen offset:1024 nt lds\n"
    "v_fma_f32 v125, v125, s56, v248\n"
    "s_branch LNX2_%=\n"
    "LND32_%=:\n"
    "s_mov_b32 s91, s89\n"
    "LNL32_%=:\n"
    "s_sub_u32 s91, s91, 1\n"
    "s_cmp_gt_i32 s91, 0\n"
    "s_cbranch_scc1 LNL32_%=\n"
    "s_branch LNR32_%=\n"
    "LND33_%=:\n"
    "s_mov_b32 s91, s89\n"
    "LNL33_%=:\n"
    "s_sub_u32 s91, s91, 1\n"
    "s_cmp_gt_i32 s91, 0\n"
    "s_cbranch_scc1 LNL33_%=\n"
    "s_branch LNR33_%=\n"
    "LND34_%=:\n"
    "s_mov_b32 s91, s89\n"
    "LNL34_%=:\n"
    "s_sub_u32 s91, s91, 1\n"
    "s_cmp_gt_i32 s91, 0\n"
    "s_cbranch_scc1 LNL34_%=\n"
    "s_branch LNR34_%=\n"
    "LND35_%=:\n"
    "s_mov_b32 s91, s89\n"
    "LNL35_%=:\n"
    "s_sub_u32 s91, s91, 1\n"
    "s_cmp_gt_i32 s91, 0\n"
    "s_cbranch_scc1 LNL35_%=\n"
    "s_branch LNR35_%=\n"
    "LNX2_%=:\n"
    "s_waitcnt lgkmcnt(10)\n"
    "v_mfma_f32_32x32x16_bf16 v[128:143], v[216:219], v[236:239], v[128:143]\n"
    "s_sub_u32 s88, s84, s90\n"
    "s_sub_u32 s85, s86, s92\n"
    "s_mov_b32 s90, s84\n"
    "s_mov_b32 s92, s86\n"
    "s_mul_i32 s87, s85, 182\n"
    "s_mul_i32 s91, s88, 10\n"
    "s_cmp_ge_u32 s91, s87\n"
    "s_cselect_b32 s88, s88, 0\n"
    "s_mul_i32 s88, s88, 48\n"
    "s_mov_b32 s89, 0\n"
    "s_mul_i32 s87, s85, 847\n"
    "s_cmp_ge_u32 s88, s87\n"
    "s_addc_u32 s89, s89, 0\n"
    "s_mul_i32 s87, s85, 912\n"
    "s_cmp_ge_u32 s88, s87\n"
    "s_addc_u32 s89, s89, 0\n"
    "s_mul_i32 s87, s85, 977\n"
    "s_cmp_ge_u32 s88, s87\n"
    "s_addc_u32 s89, s89, 0\n"
    "s_mul_i32 s87, s85, 1042\n"
    "s_cmp_ge_u32 s88, s87\n"
    "s_addc_u32 s89, s89, 0\n"
    "s_mul_i32 s87, s85, 1107\n"
    "s_cmp_ge_u32 s88, s87\n"
    "s_addc_u32 s89, s89, 0\n"
    "s_mul_i32 s87, s85, 1172\n"
    "s_cmp_ge_u32 s88, s87\n"
    "s_addc_u32 s89, s89, 0\n"
    "s_cmp_lg_u32 s89, 0\n"
    "s_cselect_b64 vcc, -1, 0\n"
    "ds_read_b64_tr_b16 v[212:213], v246 offset:20480\n"
    "ds_read_b64_tr_b16 v[214:215], v246 offset:22528\n"
    "v_exp_f32 v42, v42\n"
    "v_exp_f32 v43, v43\n"
    "v_cvt_pk_bf16_f32 v240, v40, v41\n"
    "v_add_f32 v250, v250, v40\n"
    "v_add_f32 v251, v251, v41\n"
    "v_fma_f32 v126, v126, s56, v248\n"
    "s_waitcnt lgkmcnt(10)\n"
    "v_mfma_f32_32x32x16_bf16 v[144:159], v[220:223], v[236:239], v[144:159]\n"
    "ds_read_b64_tr_b16 v[216:217], v247 offset:20480\n"
    "ds_read_b64_tr_b16 v[218:219], v247 offset:22528\n"
    "v_exp_f32 v44, v44\n"
    "v_exp_f32 v45, v45\n"
    "v_cvt_pk_bf16_f32 v241, v42, v43\n"
    "v_add_f32 v250, v250, v42\n"
    "v_add_f32 v251, v251, v43\n"
    "s_add_u32 m0, s54, 114688\n"
    "s_add_u32 s55, s53, 49152\n"
    "buffer_load_dwordx4 v254, s[36:39], s55 offen nt lds\n"
    "v_fma_f32 v127, v127, s56, v248\n"
    "s_waitcnt lgkmcnt(10)\n"
    "v_mfma_f32_32x32x16_bf16 v[160:175], v[224:227], v[236:239], v[160:175]\n"
    "ds_read_b64_tr_b16 v[220:221], v244 offset:24576\n"
    "ds_read_b64_tr_b16 v[222:223], v244 offset:26624\n"
    "v_exp_f32 v46, v46\n"
    "v_exp_f32 v47, v47\n"
    "v_cvt_pk_bf16_f32 v242, v44, v45\n"
    "v_add_f32 v250, v250, v44\n"
    "v_add_f32 v251, v251, v45\n"
    "s_waitcnt lgkmcnt(10)\n"
    "v_mfma_f32_32x32x16_bf16 v[176:191], v[228:231], v[236:239], v[176:191]\n"
    "s_cbranch_vccnz LND32_%=\n"
    "LNR32_%=:\n"
    "ds_read_b64_tr_b16 v[224:225], v245 offset:24576\n"
    "ds_read_b64_tr_b16 v[226:227], v245 offset:26624\n"
    "v_exp_f32 v48, v48\n"
    "v_exp_f32 v49, v49\n"
    "v_cvt_pk_bf16_f32 v243, v46, v47\n"
    "v_add_f32 v250, v250, v46\n"
    "v_add_f32 v251, v251, v47\n"
    "s_waitcnt lgkmcnt(10)\n"
    "v_mfma_f32_32x32x16_bf16 v[128:143], v[232:235], v[240:243], v[128:143]\n"
    "ds_read_b64_tr_b16 v[228:229], v246 offset:24576\n"
    "ds_read_b64_tr_b16 v[230:231], v246 offset:26624\n"
    "v_exp_f32 v50, v50\n"
    "v_exp_f32 v51, v51\n"
    "v_cvt_pk_bf16_f32 v236, v48, v49\n"
    "v_add_f32 v250, v250, v48\n"
    "v_add_f32 v251, v251, v49\n"
    "buffer_load_dwordx4 v249, s[36:39], s55 offen offset:1024 nt lds\n"
    "s_waitcnt lgkmcnt(10)\n"
    "v_mfma_f32_32x32x16_bf16 v[144:159], v[208:211], v[240:243], v[144:159]\n"
    "ds_read_b64_tr_b16 v[232:233], v247 offset:24576\n"
    "ds_read_b64_tr_b16 v[234:235], v247 offset:26624\n"
    "v_exp_f32 v52, v52\n"
    "v_exp_f32 v53, v53\n"
    "v_cvt_pk_bf16_f32 v237, v50, v51\n"
    "v_add_f32 v250, v250, v50\n"
    "v_add_f32 v251, v251, v51\n"
    "s_waitcnt lgkmcnt(10)\n"
    "v_mfma_f32_32x32x16_bf16 v[160:175], v[212:215], v[240:243], v[160:175]\n"
    "ds_read_b64_tr_b16 v[208:209], v244 offset:28672\n"
    "ds_read_b64_tr_b16 v[210:211], v244 offset:30720\n"
    "v_exp_f32 v54, v54\n"
    "v_exp_f32 v55, v55\n"
    "v_cvt_pk_bf16_f32 v238, v52, v53\n"
    "v_add_f32 v250, v250, v52\n"
    "v_add_f32 v251, v251, v53\n"
    "s_waitcnt lgkmcnt(10)\n"
    "v_mfma_f32_32x32x16_bf16 v[176:191], v[216:219], v[240:243], v[176:191]\n"
    "s_cbranch_vccnz LND33_%=\n"
    "LNR33_%=:\n"
    "ds_read_b64_tr_b16 v[212:213], v245 offset:28672\n"
    "ds_read_b64_tr_b16 v[214:215], v245 offset:30720\n"
    "v_exp_f32 v56, v56\n"
    "v_exp_f32 v57, v57\n"
    "v_cvt_pk_bf16_f32 v239, v54, v55\n"
    "v_add_f32 v250, v250, v54\n"
    "v_add_f32 v251, v251, v55\n"
    "s_waitcnt lgkmcnt(10)\n"
    "v_mfma_f32_32x32x16_bf16 v[128:143], v[220:223], v[236:239], v[128:143]\n"
    "ds_read_b64_tr_b16 v[216:217], v246 offset:28672\n"
    "ds_read_b64_tr_b16 v[218:219], v246 offset:30720\n"
    "v_exp_f32 v58, v58\n"
    "v_exp_f32 v59, v59\n"
    "v_cvt_pk_bf16_f32 v240, v56, v57\n"
    "v_add_f32 v250, v250, v56\n"
    "v_add_f32 v251, v251, v57\n"
    "s_waitcnt lgkmcnt(10)\n"
    "v_mfma_f32_32x32x16_bf16 v[144:159], v[224:227], v[236:239], v[144:159]\n"
    "ds_read_b64_tr_b16 v[220:221], v247 offset:28672\n"
    "ds_read_b64_tr_b16 v[222:223], v247 offset:30720\n"
    "v_exp_f32 v60, v60\n"
    "v_exp_f32 v61, v61\n"
    "v_cvt_pk_bf16_f32 v241, v58, v59\n"
    "v_add_f32 v250, v250, v58\n"
    "v_add_f32 v251, v251, v59\n"
    "s_waitcnt lgkmcnt(10)\n"
    "v_mfma_f32_32x32x16_bf16 v[160:175], v[228:231], v[236:239], v[160:175]\n"
    "s_waitcnt vmcnt(10)\n"
    "s_barrier\n"
    "ds_read_b64_tr_b16 v[224:225], v244 offset:32768\n"
    "ds_read_b64_tr_b16 v[226:227], v244 offset:34816\n"
    "v_exp_f32 v62, v62\n"
    "v_exp_f32 v63, v63\n"
    "v_cvt_pk_bf16_f32 v242, v60, v61\n"
    "v_add_f32 v250, v250, v60\n"
    "v_add_f32 v251, v251, v61\n"
    "s_waitcnt lgkmcnt(10)\n"
    "v_mfma_f32_32x32x16_bf16 v[176:191], v[232:235], v[236:239], v[176:191]\n"
    "s_cbranch_vccnz LND34_%=\n"
    "LNR34_%=:\n"
    "ds_read_b64_tr_b16 v[228:229], v245 offset:32768\n"
    "ds_read_b64_tr_b16 v[230:231], v245 offset:34816\n"
    "v_exp_f32 v64, v64\n"
    "v_exp_f32 v65, v65\n"
    "v_cvt_pk_bf16_f32 v243, v62, v63\n"
    "v_add_f32 v250, v250, v62\n"
    "v_add_f32 v251, v251, v63\n"
    "s_waitcnt lgkmcnt(10)\n"
    "v_mfma_f32_32x32x16_bf16 v[128:143], v[208:211], v[240:243], v[128:143]\n"
    "s_memtime s[84:85]\n"
    "s_memrealtime s[86:87]\n"
    "ds_read_b64_tr_b16 v[232:233], v246 offset:32768\n"
    "ds_read_b64_tr_b16 v[234:235], v246 offset:34816\n"
    "v_exp_f32 v66, v66\n"
    "v_exp_f32 v67, v67\n"
    "v_cvt_pk_bf16_f32 v236, v64, v65\n"
    "v_add_f32 v250, v250, v64\n"
    "v_add_f32 v251, v251, v65\n"
    "s_add_u32 m0, s54, 0\n"
    "s_add_u32 s55, s53, 0\n"
    "buffer_load_dwordx4 v254, s[40:43], s55 offen nt lds\n"
    "s_waitcnt lgkmcnt(10)\n"
    "v_mfma_f32_32x32x16_bf16 v[144:159], v[212:215], v[240:243], v[144:159]\n"
    "ds_read_b64_tr_b16 v[208:209], v247 offset:32768\n"
    "ds_read_b64_tr_b16 v[210:211], v247 offset:34816\n"
    "v_exp_f32 v68, v68\n"
    "v_exp_f32 v69, v69\n"
    "v_cvt_pk_bf16_f32 v237, v66, v67\n"
    "v_add_f32 v250, v250, v66\n"
    "v_add_f32 v251, v251, v67\n"
    "s_waitcnt lgkmcnt(10)\n"
    "v_mfma_f32_32x32x16_bf16 v[160:175], v[216:219], v[240:243], v[160:175]\n"
    "ds_read_b64_tr_b16 v[212:213], v244 offset:36864\n"
    "ds_read_b64_tr_b16 v[214:215], v244 offset:38912\n"
    "v_exp_f32 v70, v70\n"
    "v_exp_f32 v71, v71\n"
    "v_cvt_pk_bf16_f32 v238, v68, v69\n"
    "v_add_f32 v250, v250, v68\n"
    "v_add_f32 v251, v251, v69\n"
    "s_waitcnt lgkmcnt(10)\n"
    "v_mfma_f32_32x32x16_bf16 v[176:191], v[220:223], v[240:243], v[176:191]\n"
    "s_cbranch_vccnz LND35_%=\n"
    "LNR35_%=:\n"
    "ds_read_b64_tr_b16 v[216:217], v245 offset:36864\n"
    "ds_read_b64_tr_b16 v[218:219], v245 offset:38912\n"
    "v_exp_f32 v72, v72\n"
    "v_exp_f32 v73, v73\n"
    "v_cvt_pk_bf16_f32 v239, v70, v71\n"
    "v_add_f32 v250, v250, v70\n"
    "v_add_f32 v251, v251, v71\n"
    "s_branch LNX3_%=\n"
    "LND48_%=:\n"
    "s_mov_b32 s91, s89\n"
    "LNL48_%=:\n"
    "s_sub_u32 s91, s91, 1\n"
    "s_cmp_gt_i32 s91, 0\n"
    "s_cbranch_scc1 LNL48_%=\n"
    "s_branch LNR48_%=\n"
    "LND49_%=:\n"
    "s_mov_b32 s91, s89\n"
    "LNL49_%=:\n"
    "s_sub_u32 s91, s91, 1\n"
    "s_cmp_gt_i32 s91, 0\n"
    "s_cbranch_scc1 LNL49_%=\n"
    "s_branch LNR49_%=\n"
    "LND50_%=:\n"
    "s_mov_b32 s91, s89\n"
    "LNL50_%=:\n"
    "s_sub_u32 s91, s91, 1\n"
    "s_cmp_gt_i32 s91, 0\n"
    "s_cbranch_scc1 LNL50_%=\n"
    "s_branch LNR50_%=\n"
    "LND51_%=:\n"
    "s_mov_b32 s91, s89\n"
    "LNL51_%=:\n"
    "s_sub_u32 s91, s91, 1\n"
    "s_cmp_gt_i32 s91, 0\n"
    "s_cbranch_scc1 LNL51_%=\n"
    "s_branch LNR51_%=\n"
    "LNX3_%=:\n"
    "s_waitcnt lgkmcnt(10)\n"
    "v_mfma_f32_32x32x16_bf16 v[128:143], v[224:227], v[236:239], v[128:143]\n"
    "s_sub_u32 s88, s84, s90\n"
    "s_sub_u32 s85, s86, s92\n"
    "s_mov_b32 s90, s84\n"
    "s_mov_b32 s92, s86\n"
    "s_mul_i32 s87, s85, 182\n"
    "s_mul_i32 s91, s88, 10\n"
    "s_cmp_ge_u32 s91, s87\n"
    "s_cselect_b32 s88, s88, 0\n"
    "s_mul_i32 s88, s88, 48\n"
    "s_mov_b32 s89, 0\n"
    "s_mul_i32 s87, s85, 847\n"
    "s_cmp_ge_u32 s88, s87\n"
    "s_addc_u32 s89, s89, 0\n"
    "s_mul_i32 s87, s85, 912\n"
    "s_cmp_ge_u32 s88, s87\n"
    "s_addc_u32 s89, s89, 0\n"
    "s_mul_i32 s87, s85, 977\n"
    "s_cmp_ge_u32 s88, s87\n"
    "s_addc_u32 s89, s89, 0\n"
    "s_mul_i32 s87, s85, 1042\n"
    "s_cmp_ge_u32 s88, s87\n"
    "s_addc_u32 s89, s89, 0\n"
    "s_mul_i32 s87, s85, 1107\n"
    "s_cmp_ge_u32 s88, s87\n"
    "s_addc_u32 s89, s89, 0\n"
    "s_mul_i32 s87, s85, 1172\n"
    "s_cmp_ge_u32 s88, s87\n"
    "s_addc_u32 s89, s89, 0\n"
    "s_cmp_lg_u32 s89, 0\n"
    "s_cselect_b64 vcc, -1, 0\n"
    "ds_read_b64_tr_b16 v[220:221], v246 offset:36864\n"
    "ds_read_b64_tr_b16 v[222:223], v246 offset:38912\n"
    "v_exp_f32 v74, v74\n"
    "v_exp_f32 v75, v75\n"
    "v_cvt_pk_bf16_f32 v240, v72, v73\n"
    "v_add_f32 v250, v250, v72\n"
    "v_add_f32 v251, v251, v73\n"
    "s_waitcnt lgkmcnt(10)\n"
    "v_mfma_f32_32x32x16_bf16 v[144:159], v[228:231], v[236:239], v[144:159]\n"
    "ds_read_b64_tr_b16 v[224:225], v247 offset:36864\n"
    "ds_read_b64_tr_b16 v[226:227], v247 offset:38912\n"
    "v_exp_f32 v76, v76\n"
    "v_exp_f32 v77, v77\n"
    "v_cvt_pk_bf16_f32 v241, v74, v75\n"
    "v_add_f32 v250, v250, v74\n"
    "v_add_f32 v251, v251, v75\n"
    "s_waitcnt lgkmcnt(10)\n"
    "v_mfma_f32_32x32x16_bf16 v[160:175], v[232:235], v[236:239], v[160:175]\n"
    "ds_read_b64_tr_b16 v[228:229], v244 offset:40960\n"
    "ds_read_b64_tr_b16 v[230:231], v244 offset:43008\n"
    "v_exp_f32 v78, v78\n"
    "v_exp_f32 v79, v79\n"
    "v_cvt_pk_bf16_f32 v242, v76, v77\n"
    "v_add_f32 v250, v250, v76\n"
    "v_add_f32 v251, v251, v77\n"
    "s_waitcnt lgkmcnt(10)\n"
    "v_mfma_f32_32x32x16_bf16 v[176:191], v[208:211], v[236:239], v[176:191]\n"
    "s_cbranch_vccnz LND48_%=\n"
    "LNR48_%=:\n"
    "ds_read_b64_tr_b16 v[232:233], v245 offset:40960\n"
    "ds_read_b64_tr_b16 v[234:235], v245 offset:43008\n"
    "v_exp_f32 v80, v80\n"
    "v_exp_f32 v81, v81\n"
    "v_cvt_pk_bf16_f32 v243, v78, v79\n"
    "v_add_f32 v250, v250, v78\n"
    "v_add_f32 v251, v251, v79\n"
    "buffer_load_dwordx4 v249, s[40:43], s55 offen offset:1024 nt lds\n"
    "s_waitcnt lgkmcnt(10)\n"
    "v_mfma_f32_32x32x16_bf16 v[128:143], v[212:215], v[240:243], v[128:143]\n"
    "ds_read_b64_tr_b16 v[208:209], v246 offset:40960\n"
    "ds_read_b64_tr_b16 v[210:211], v246 offset:43008\n"
    "v_exp_f32 v82, v82\n"
    "v_exp_f32 v83, v83\n"
    "v_cvt_pk_bf16_f32 v236, v80, v81\n"
    "v_add_f32 v250, v250, v80\n"
    "v_add_f32 v251, v251, v81\n"
    "s_waitcnt lgkmcnt(10)\n"
    "v_mfma_f32_32x32x16_bf16 v[144:159], v[216:219], v[240:243], v[144:159]\n"
    "ds_read_b64_tr_b16 v[212:213], v247 offset:40960\n"
    "ds_read_b64_tr_b16 v[214:215], v247 offset:43008\n"
    "v_exp_f32 v84, v84\n"
    "v_exp_f32 v85, v85\n"
    "v_cvt_pk_bf16_f32 v237, v82, v83\n"
    "v_add_f32 v250, v250, v82\n"
    "v_add_f32 v251, v251, v83\n"
    "s_waitcnt lgkmcnt(10)\n"
    "v_mfma_f32_32x32x16_bf16 v[160:175], v[220:223], v[240:243], v[160:175]\n"
    "ds_read_b64_tr_b16 v[216:217], v244 offset:45056\n"
    "ds_read_b64_tr_b16 v[218:219], v244 offset:47104\n"
    "v_exp_f32 v86, v86\n"
    "v_exp_f32 v87, v87\n"
    "v_cvt_pk_bf16_f32 v238, v84, v85\n"
    "v_add_f32 v250, v250, v84\n"
    "v_add_f32 v251, v251, v85\n"
    "s_waitcnt lgkmcnt(10)\n"
    "v_mfma_f32_32x32x16_bf16 v[176:191], v[224:227], v[240:243], v[176:191]\n"
    "s_cbranch_vccnz LND49_%=\n"
    "LNR49_%=:\n"
    "ds_read_b64_tr_b16 v[220:221], v245 offset:45056\n"
    "ds_read_b64_tr_b16 v[222:223], v245 offset:47104\n"
    "v_exp_f32 v88, v88\n"
    "v_exp_f32 v89, v89\n"
    "v_cvt_pk_bf16_f32 v239, v86, v87\n"
    "v_add_f32 v250, v250, v86\n"
    "v_add_f32 v251, v251, v87\n"
    "s_waitcnt lgkmcnt(10)\n"
    "v_mfma_f32_32x32x16_bf16 v[128:143], v[228:231], v[236:239], v[128:143]\n"
    "ds_read_b64_tr_b16 v[224:225], v246 offset:45056\n"
    "ds_read_b64_tr_b16 v[226:227], v246 offset:47104\n"
    "v_exp_f32 v90, v90\n"
    "v_exp_f32 v91, v91\n"
    "v_cvt_pk_bf16_f32 v240, v88, v89\n"
    "v_add_f32 v250, v250, v88\n"
    "v_add_f32 v251, v251, v89\n"
    "s_waitcnt lgkmcnt(10)\n"
    "v_mfma_f32_32x32x16_bf16 v[144:159], v[232:235], v[236:239], v[144:159]\n"
    "ds_read_b64_tr_b16 v[228:229], v247 offset:45056\n"
    "ds_read_b64_tr_b16 v[230:231], v247 offset:47104\n"
    "v_exp_f32 v92, v92\n"
    "v_exp_f32 v93, v93\n"
    "v_cvt_pk_bf16_f32 v241, v90, v91\n"
    "v_add_f32 v250, v250, v90\n"
    "v_add_f32 v251, v251, v91\n"
    "s_waitcnt lgkmcnt(10)\n"
    "v_mfma_f32_32x32x16_bf16 v[160:175], v[208:211], v[236:239], v[160:175]\n"
    "s_waitcnt vmcnt(10)\n"
    "s_barrier\n"
    "ds_read_b64_tr_b16 v[232:233], v244 offset:49152\n"
    "ds_read_b64_tr_b16 v[234:235], v244 offset:51200\n"
    "v_exp_f32 v94, v94\n"
    "v_exp_f32 v95, v95\n"
    "v_cvt_pk_bf16_f32 v242, v92, v93\n"
    "v_add_f32 v250, v250, v92\n"
    "v_add_f32 v251, v251, v93\n"
    "s_waitcnt lgkmcnt(10)\n"
    "v_mfma_f32_32x32x16_bf16 v[176:191], v[212:215], v[236:239], v[176:191]\n"
    "s_cbranch_vccnz LND50_%=\n"
    "LNR50_%=:\n"
    "ds_read_b64_tr_b16 v[208:209], v245 offset:49152\n"
    "ds_read_b64_tr_b16 v[210:211], v245 offset:51200\n"
    "v_exp_f32 v96, v96\n"
    "v_exp_f32 v97, v97\n"
    "v_cvt_pk_bf16_f32 v243, v94, v95\n"
    "v_add_f32 v250, v250, v94\n"
    "v_add_f32 v251, v251, v95\n"
    "s_waitcnt lgkmcnt(10)\n"
    "v_mfma_f32_32x32x16_bf16 v[128:143], v[216:219], v[240:243], v[128:143]\n"
    "s_memtime s[84:85]\n"
    "s_memrealtime s[86:87]\n"
    "ds_read_b64_tr_b16 v[212:213], v246 offset:49152\n"
    "ds_read_b64_tr_b16 v[214:215], v246 offset:51200\n"
    "v_exp_f32 v98, v98\n"
    "v_exp_f32 v99, v99\n"
    "v_cvt_pk_bf16_f32 v236, v96, v97\n"
    "v_add_f32 v250, v250, v96\n"
    "v_add_f32 v251, v251, v97\n"
    "s_add_u32 m0, s54, 16384\n"
    "s_add_u32 s55, s53, 16384\n"
    "buffer_load_dwordx4 v254, s[40:43], s55 offen nt lds\n"
    "s_waitcnt lgkmcnt(10)\n"
    "v_mfma_f32_32x32x16_bf16 v[144:159], v[220:223], v[240:243], v[144:159]\n"
    "ds_read_b64_tr_b16 v[216:217], v247 offset:49152\n"
    "ds_read_b64_tr_b16 v[218:219], v247 offset:51200\n"
    "v_exp_f32 v100, v100\n"
    "v_exp_f32 v101, v101\n"
    "v_cvt_pk_bf16_f32 v237, v98, v99\n"
    "v_add_f32 v250, v250, v98\n"
    "v_add_f32 v251, v251, v99\n"
    "s_waitcnt lgkmcnt(10)\n"
    "v_mfma_f32_32x32x16_bf16 v[160:175], v[224:227], v[240:243], v[160:175]\n"
    "ds_read_b64_tr_b16 v[220:221], v244 offset:53248\n"
    "ds_read_b64_tr_b16 v[222:223], v244 offset:55296\n"
    "v_exp_f32 v102, v102\n"
    "v_exp_f32 v103, v103\n"
    "v_cvt_pk_bf16_f32 v238, v100, v101\n"
    "v_add_f32 v250, v250, v100\n"
    "v_add_f32 v251, v251, v101\n"
    "s_waitcnt lgkmcnt(10)\n"
    "v_mfma_f32_32x32x16_bf16 v[176:191], v[228:231], v[240:243], v[176:191]\n"
    "s_cbranch_vccnz LND51_%=\n"
    "LNR51_%=:\n"
    "ds_read_b64_tr_b16 v[224:225], v245 offset:53248\n"
    "ds_read_b64_tr_b16 v[226:227], v245 offset:55296\n"
    "v_exp_f32 v104, v104\n"
    "v_exp_f32 v105, v105\n"
    "v_cvt_pk_bf16_f32 v239, v102, v103\n"
    "v_add_f32 v250, v250, v102\n"
    "v_add_f32 v251, v251, v103\n"
    "s_branch LNX4_%=\n"
    "LND64_%=:\n"
    "s_mov_b32 s91, s89\n"
    "LNL64_%=:\n"
    "s_sub_u32 s91, s91, 1\n"
    "s_cmp_gt_i32 s91, 0\n"
    "s_cbranch_scc1 LNL64_%=\n"
    "s_branch LNR64_%=\n"
    "LND65_%=:\n"
    "s_mov_b32 s91, s89\n"
    "LNL65_%=:\n"
    "s_sub_u32 s91, s91, 1\n"
    "s_cmp_gt_i32 s91, 0\n"
    "s_cbranch_scc1 LNL65_%=\n"
    "s_branch LNR65_%=\n"
    "LND66_%=:\n"
    "s_mov_b32 s91, s89\n"
    "LNL66_%=:\n"
    "s_sub_u32 s91, s91, 1\n"
    "s_cmp_gt_i32 s91, 0\n"
    "s_cbranch_scc1 LNL66_%=\n"
    "s_branch LNR66_%=\n"
    "LND67_%=:\n"
    "s_mov_b32 s91, s89\n"
    "LNL67_%=:\n"
    "s_sub_u32 s91, s91, 1\n"
    "s_cmp_gt_i32 s91, 0\n"
    "s_cbranch_scc1 LNL67_%=\n"
    "s_branch LNR67_%=\n"
    "LNX4_%=:\n"
    "s_waitcnt lgkmcnt(10)\n"
    "v_mfma_f32_32x32x16_bf16 v[128:143], v[232:235], v[236:239], v[128:143]\n"
    "s_sub_u32 s88, s84, s90\n"
    "s_sub_u32 s85, s86, s92\n"
    "s_mov_b32 s90, s84\n"
    "s_mov_b32 s92, s86\n"
    "s_mul_i32 s87, s85, 182\n"
    "s_mul_i32 s91, s88, 10\n"
    "s_cmp_ge_u32 s91, s87\n"
    "s_cselect_b32 s88, s88, 0\n"
    "s_mul_i32 s88, s88, 48\n"
    "s_mov_b32 s89, 0\n"
    "s_mul_i32 s87, s85, 847\n"
    "s_cmp_ge_u32 s88, s87\n"
    "s_addc_u32 s89, s89, 0\n"
    "s_mul_i32 s87, s85, 912\n"
    "s_cmp_ge_u32 s88, s87\n"
    "s_addc_u32 s89, s89, 0\n"
    "s_mul_i32 s87, s85, 977\n"
    "s_cmp_ge_u32 s88, s87\n"
    "s_addc_u32 s89, s89, 0\n"
    "s_mul_i32 s87, s85, 1042\n"
    "s_cmp_ge_u32 s88, s87\n"
    "s_addc_u32 s89, s89, 0\n"
    "s_mul_i32 s87, s85, 1107\n"
    "s_cmp_ge_u32 s88, s87\n"
    "s_addc_u32 s89, s89, 0\n"
    "s_mul_i32 s87, s85, 1172\n"
    "s_cmp_ge_u32 s88, s87\n"
    "s_addc_u32 s89, s89, 0\n"
    "s_cmp_lg_u32 s89, 0\n"
    "s_cselect_b64 vcc, -1, 0\n"
    "ds_read_b64_tr_b16 v[228:229], v246 offset:53248\n"
    "ds_read_b64_tr_b16 v[230:231], v246 offset:55296\n"
    "v_exp_f32 v106, v106\n"
    "v_exp_f32 v107, v107\n"
    "v_cvt_pk_bf16_f32 v240, v104, v105\n"
    "v_add_f32 v250, v250, v104\n"
    "v_add_f32 v251, v251, v105\n"
    "s_waitcnt lgkmcnt(10)\n"
    "v_mfma_f32_32x32x16_bf16 v[144:159], v[208:211], v[236:239], v[144:159]\n"
    "ds_read_b64_tr_b16 v[232:233], v247 offset:53248\n"
    "ds_read_b64_tr_b16 v[234:235], v247 offset:55296\n"
    "v_exp_f32 v108, v108\n"
    "v_exp_f32 v109, v109\n"
    "v_cvt_pk_bf16_f32 v241, v106, v107\n"
    "v_add_f32 v250, v250, v106\n"
    "v_add_f32 v251, v251, v107\n"
    "s_waitcnt lgkmcnt(10)\n"
    "v_mfma_f32_32x32x16_bf16 v[160:175], v[212:215], v[236:239], v[160:175]\n"
    "ds_read_b64_tr_b16 v[208:209], v244 offset:57344\n"
    "ds_read_b64_tr_b16 v[210:211], v244 offset:59392\n"
    "v_exp_f32 v110, v110\n"
    "v_exp_f32 v111, v111\n"
    "v_cvt_pk_bf16_f32 v242, v108, v109\n"
    "v_add_f32 v250, v250, v108\n"
    "v_add_f32 v251, v251, v109\n"
    "s_waitcnt lgkmcnt(10)\n"
    "v_mfma_f32_32x32x16_bf16 v[176:191], v[216:219], v[236:239], v[176:191]\n"
    "s_cbranch_vccnz LND64_%=\n"
    "LNR64_%=:\n"
    "ds_read_b64_tr_b16 v[212:213], v245 offset:57344\n"
    "ds_read_b64_tr_b16 v[214:215], v245 offset:59392\n"
    "v_exp_f32 v112, v112\n"
    "v_exp_f32 v113, v113\n"
    "v_cvt_pk_bf16_f32 v243, v110, v111\n"
    "v_add_f32 v250, v250, v110\n"
    "v_add_f32 v251, v251, v111\n"
    "buffer_load_dwordx4 v249, s[40:43], s55 offen offset:1024 nt lds\n"
    "s_waitcnt lgkmcnt(10)\n"
    "v_mfma_f32_32x32x16_bf16 v[128:143], v[220:223], v[240:243], v[128:143]\n"
    "ds_read_b64_tr_b16 v[216:217], v246 offset:57344\n"
    "ds_read_b64_tr_b16 v[218:219], v246 offset:59392\n"
    "v_exp_f32 v114, v114\n"
    "v_exp_f32 v115, v115\n"
    "v_cvt_pk_bf16_f32 v236, v112, v113\n"
    "v_add_f32 v250, v250, v112\n"
    "v_add_f32 v251, v251, v113\n"
    "s_waitcnt lgkmcnt(10)\n"
    "v_mfma_f32_32x32x16_bf16 v[144:159], v[224:227], v[240:243], v[144:159]\n"
    "ds_read_b64_tr_b16 v[220:221], v247 offset:57344\n"
    "ds_read_b64_tr_b16 v[222:223], v247 offset:59392\n"
    "v_exp_f32 v116, v116\n"
    "v_exp_f32 v117, v117\n"
    "v_cvt_pk_bf16_f32 v237, v114, v115\n"
    "v_add_f32 v250, v250, v114\n"
    "v_add_f32 v251, v251, v115\n"
    "s_waitcnt lgkmcnt(10)\n"
    "v_mfma_f32_32x32x16_bf16 v[160:175], v[228:231], v[240:243], v[160:175]\n"
    "ds_read_b64_tr_b16 v[224:225], v244 offset:61440\n"
    "ds_read_b64_tr_b16 v[226:227], v244 offset:63488\n"
    "v_exp_f32 v118, v118\n"
    "v_exp_f32 v119, v119\n"
    "v_cvt_pk_bf16_f32 v238, v116, v117\n"
    "v_add_f32 v250, v250, v116\n"
    "v_add_f32 v251, v251, v117\n"
    "s_waitcnt lgkmcnt(10)\n"
    "v_mfma_f32_32x32x16_bf16 v[176:191], v[232:235], v[240:243], v[176:191]\n"
    "s_cbranch_vccnz LND65_%=\n"
    "LNR65_%=:\n"
    "ds_read_b64_tr_b16 v[228:229], v245 offset:61440\n"
    "ds_read_b64_tr_b16 v[230:231], v245 offset:63488\n"
    "v_exp_f32 v120, v120\n"
    "v_exp_f32 v121, v121\n"
    "v_cvt_pk_bf16_f32 v239, v118, v119\n"
    "v_add_f32 v250, v250, v118\n"
    "v_add_f32 v251, v251, v119\n"
    "s_waitcnt lgkmcnt(10)\n"
    "v_mfma_f32_32x32x16_bf16 v[128:143], v[208:211], v[236:239], v[128:143]\n"
    "ds_read_b64_tr_b16 v[232:233], v246 offset:61440\n"
    "ds_read_b64_tr_b16 v[234:235], v246 offset:63488\n"
    "v_exp_f32 v122, v122\n"
    "v_exp_f32 v123, v123\n"
    "v_cvt_pk_bf16_f32 v240, v120, v121\n"
    "v_add_f32 v250, v250, v120\n"
    "v_add_f32 v251, v251, v121\n"
    "s_waitcnt lgkmcnt(10)\n"
    "v_mfma_f32_32x32x16_bf16 v[144:159], v[212:215], v[236:239], v[144:159]\n"
    "ds_read_b64_tr_b16 v[208:209], v247 offset:61440\n"
    "ds_read_b64_tr_b16 v[210:211], v247 offset:63488\n"
    "v_exp_f32 v124, v124\n"
    "v_exp_f32 v125, v125\n"
    "v_cvt_pk_bf16_f32 v241, v122, v123\n"
    "v_add_f32 v250, v250, v122\n"
    "v_add_f32 v251, v251, v123\n"
    "s_waitcnt lgkmcnt(10)\n"
    "v_mfma_f32_32x32x16_bf16 v[160:175], v[216:219], v[236:239], v[160:175]\n"
    "v_exp_f32 v126, v126\n"
    "v_exp_f32 v127, v127\n"
    "v_cvt_pk_bf16_f32 v242, v124, v125\n"
    "v_add_f32 v250, v250, v124\n"
    "v_add_f32 v251, v251, v125\n"
    "s_waitcnt lgkmcnt(8)\n"
    "v_mfma_f32_32x32x16_bf16 v[176:191], v[220:223], v[236:239], v[176:191]\n"
    "s_cbranch_vccnz LND66_%=\n"
    "LNR66_%=:\n"
    "v_cvt_pk_bf16_f32 v243, v126, v127\n"
    "v_add_f32 v250, v250, v126\n"
    "v_add_f32 v251, v251, v127\n"
    "s_waitcnt lgkmcnt(6)\n"
    "v_mfma_f32_32x32x16_bf16 v[128:143], v[224:227], v[240:243], v[128:143]\n"
    "v_add_f32 v250, v250, v251\n"
    "v_mov_b32 v251, v250\n"
    "s_nop 1\n"
    "v_permlane32_swap_b32 v250, v251\n"
    "v_add_f32 v250, v250, v251\n"
    "v_cmp_nge_f32 vcc, s57, v250\n"
    "v_rcp_f32 v250, v250\n"
    "s_nop 1\n"
    "s_nop 0\n"
    "s_cmp_lg_u64 vcc, 0\n"
    "s_cselect_b32 s62, 1, 0\n"
    "s_or_b32 s83, s83, s62\n"
    "s_mov_b64 vcc, 0\n"
    "s_waitcnt lgkmcnt(4)\n"
    "v_mfma_f32_32x32x16_bf16 v[144:159], v[228:231], v[240:243], v[144:159]\n"
    "s_waitcnt lgkmcnt(2)\n"
    "v_mfma_f32_32x32x16_bf16 v[160:175], v[232:235], v[240:243], v[160:175]\n"
    "s_waitcnt lgkmcnt(0)\n"
    "v_mfma_f32_32x32x16_bf16 v[176:191], v[208:211], v[240:243], v[176:191]\n"
    "s_cbranch_vccnz LND67_%=\n"
    "LNR67_%=:\n"
    "s_waitcnt vmcnt(4) lgkmcnt(0)\n"
    "s_barrier\n"
    "s_add_u32 m0, s54, 32768\n"
    "s_add_u32 s55, s53, 32768\n"
    "buffer_load_dwordx4 v254, s[40:43], s55 offen nt lds\n"
    "buffer_load_dwordx4 v249, s[40:43], s55 offen offset:1024 nt lds\n"
    "s_add_u32 m0, s54, 49152\n"
    "s_add_u32 s55, s53, 49152\n"
    "buffer_load_dwordx4 v254, s[40:43], s55 offen nt lds\n"
    "buffer_load_dwordx4 v249, s[40:43], s55 offen offset:1024 nt lds\n"
    "s_memtime s[84:85]\n"
    "s_memrealtime s[86:87]\n"
    "v_pk_mul_f32 v[128:129], v[128:129], v[250:251] op_sel_hi:[1,0]\n"
    "v_pk_mul_f32 v[130:131], v[130:131], v[250:251] op_sel_hi:[1,0]\n"
    "v_cvt_pk_bf16_f32 v192, v128, v129\n"
    "v_cvt_pk_bf16_f32 v193, v130, v131\n"
    "v_pk_mul_f32 v[132:133], v[132:133], v[250:251] op_sel_hi:[1,0]\n"
    "v_pk_mul_f32 v[134:135], v[134:135], v[250:251] op_sel_hi:[1,0]\n"
    "v_cvt_pk_bf16_f32 v194, v132, v133\n"
    "v_cvt_pk_bf16_f32 v195, v134, v135\n"
    "v_pk_mul_f32 v[136:137], v[136:137], v[250:251] op_sel_hi:[1,0]\n"
    "v_pk_mul_f32 v[138:139], v[138:139], v[250:251] op_sel_hi:[1,0]\n"
    "v_cvt_pk_bf16_f32 v196, v136, v137\n"
    "v_cvt_pk_bf16_f32 v197, v138, v139\n"
    "v_pk_mul_f32 v[140:141], v[140:141], v[250:251] op_sel_hi:[1,0]\n"
    "v_pk_mul_f32 v[142:143], v[142:143], v[250:251] op_sel_hi:[1,0]\n"
    "v_cvt_pk_bf16_f32 v198, v140, v141\n"
    "v_cvt_pk_bf16_f32 v199, v142, v143\n"
    "v_pk_mul_f32 v[144:145], v[144:145], v[250:251] op_sel_hi:[1,0]\n"
    "v_pk_mul_f32 v[146:147], v[146:147], v[250:251] op_sel_hi:[1,0]\n"
    "v_cvt_pk_bf16_f32 v200, v144, v145\n"
    "v_cvt_pk_bf16_f32 v201, v146, v147\n"
    "v_pk_mul_f32 v[148:149], v[148:149], v[250:251] op_sel_hi:[1,0]\n"
    "v_pk_mul_f32 v[150:151], v[150:151], v[250:251] op_sel_hi:[1,0]\n"
    "v_cvt_pk_bf16_f32 v202, v148, v149\n"
    "v_cvt_pk_bf16_f32 v203, v150, v151\n"
    "v_pk_mul_f32 v[152:153], v[152:153], v[250:251] op_sel_hi:[1,0]\n"
    "v_pk_mul_f32 v[154:155], v[154:155], v[250:251] op_sel_hi:[1,0]\n"
    "v_cvt_pk_bf16_f32 v204, v152, v153\n"
    "v_cvt_pk_bf16_f32 v205, v154, v155\n"
    "v_pk_mul_f32 v[156:157], v[156:157], v[250:251] op_sel_hi:[1,0]\n"
    "v_pk_mul_f32 v[158:159], v[158:159], v[250:251] op_sel_hi:[1,0]\n"
    "v_cvt_pk_bf16_f32 v206, v156, v157\n"
    "v_cvt_pk_bf16_f32 v207, v158, v159\n"
    "v_xor_b32 v240, 0, v252\n"
    "v_xor_b32 v241, 32, v252\n"
    "v_xor_b32 v242, 64, v252\n"
    "v_xor_b32 v243, 96, v252\n"
    "v_xor_b32 v244, 128, v252\n"
    "v_xor_b32 v245, 160, v252\n"
    "v_xor_b32 v246, 192, v252\n"
    "v_xor_b32 v247, 224, v252\n"
    "s_lshr_b32 s62, s58, 1\n"
    "s_mov_b32 s63, 65536\n"
    "s_cmp_eq_u32 s62, 1\n"
    "s_cselect_b32 s63, 81920, s63\n"
    "s_cmp_eq_u32 s62, 2\n"
    "s_cselect_b32 s63, 98304, s63\n"
    "s_cmp_eq_u32 s62, 3\n"
    "s_cselect_b32 s63, 114688, s63\n"
    "s_and_b32 s62, s58, 1\n"
    "s_lshl_b32 s62, s62, 13\n"
    "s_add_u32 s63, s63, s62\n"
    "v_and_b32 v224, 0xffff, v252\n"
    "v_add_u32 v224, s63, v224\n"
    "v_xor_b32 v225, 32, v224\n"
    "v_xor_b32 v226, 64, v224\n"
    "v_xor_b32 v227, 96, v224\n"
    "v_xor_b32 v228, 128, v224\n"
    "v_xor_b32 v229, 160, v224\n"
    "v_xor_b32 v230, 192, v224\n"
    "v_xor_b32 v231, 224, v224\n"
    "ds_read_b128 v[128:131], v224 offset:0\n"
    "ds_read_b128 v[132:135], v225 offset:0\n"
    "ds_read_b128 v[136:139], v226 offset:0\n"
    "ds_read_b128 v[140:143], v227 offset:0\n"
    "ds_read_b128 v[144:147], v228 offset:0\n"
    "ds_read_b128 v[148:151], v229 offset:0\n"
    "ds_read_b128 v[152:155], v230 offset:0\n"
    "ds_read_b128 v[156:159], v231 offset:0\n"
    "v_pk_mul_f32 v[160:161], v[160:161], v[250:251] op_sel_hi:[1,0]\n"
    "v_pk_mul_f32 v[162:163], v[162:163], v[250:251] op_sel_hi:[1,0]\n"
    "v_cvt_pk_bf16_f32 v208, v160, v161\n"
    "v_cvt_pk_bf16_f32 v209, v162, v163\n"
    "v_pk_mul_f32 v[164:165], v[164:165], v[250:251] op_sel_hi:[1,0]\n"
    "v_pk_mul_f32 v[166:167], v[166:167], v[250:251] op_sel_hi:[1,0]\n"
    "v_cvt_pk_bf16_f32 v210, v164, v165\n"
    "v_cvt_pk_bf16_f32 v211, v166, v167\n"
    "v_pk_mul_f32 v[168:169], v[168:169], v[250:251] op_sel_hi:[1,0]\n"
    "v_pk_mul_f32 v[170:171], v[170:171], v[250:251] op_sel_hi:[1,0]\n"
    "v_cvt_pk_bf16_f32 v212, v168, v169\n"
    "v_cvt_pk_bf16_f32 v213, v170, v171\n"
    "v_pk_mul_f32 v[172:173], v[172:173], v[250:251] op_sel_hi:[1,0]\n"
    "v_pk_mul_f32 v[174:175], v[174:175], v[250:251] op_sel_hi:[1,0]\n"
    "v_cvt_pk_bf16_f32 v214, v172, v173\n"
    "v_cvt_pk_bf16_f32 v215, v174, v175\n"
    "v_pk_mul_f32 v[176:177], v[176:177], v[250:251] op_sel_hi:[1,0]\n"
    "v_pk_mul_f32 v[178:179], v[178:179], v[250:251] op_sel_hi:[1,0]\n"
    "v_cvt_pk_bf16_f32 v216, v176, v177\n"
    "v_cvt_pk_bf16_f32 v217, v178, v179\n"
    "v_pk_mul_f32 v[180:181], v[180:181], v[250:251] op_sel_hi:[1,0]\n"
    "v_pk_mul_f32 v[182:183], v[182:183], v[250:251] op_sel_hi:[1,0]\n"
    "v_cvt_pk_bf16_f32 v218, v180, v181\n"
    "v_cvt_pk_bf16_f32 v219, v182, v183\n"
    "v_pk_mul_f32 v[184:185], v[184:185], v[250:251] op_sel_hi:[1,0]\n"
    "v_pk_mul_f32 v[186:187], v[186:187], v[250:251] op_sel_hi:[1,0]\n"
    "v_cvt_pk_bf16_f32 v220, v184, v185\n"
    "v_cvt_pk_bf16_f32 v221, v186, v187\n"
    "v_pk_mul_f32 v[188:189], v[188:189], v[250:251] op_sel_hi:[1,0]\n"
    "v_pk_mul_f32 v[190:191], v[190:191], v[250:251] op_sel_hi:[1,0]\n"
    "v_cvt_pk_bf16_f32 v222, v188, v189\n"
    "v_cvt_pk_bf16_f32 v223, v190, v191\n"
    "s_waitcnt vmcnt(6) lgkmcnt(0)\n"
    "s_barrier\n"
    "ds_read_b128 v[160:163], v240 offset:0\n"
    "ds_read_b128 v[164:167], v241 offset:0\n"
    "ds_read_b128 v[168:171], v242 offset:0\n"
    "ds_read_b128 v[172:175], v243 offset:0\n"
    "ds_read_b128 v[176:179], v244 offset:0\n"
    "ds_read_b128 v[180:183], v245 offset:0\n"
    "ds_read_b128 v[184:187], v246 offset:0\n"
    "s_branch LNX5_%=\n"
    "LND80_%=:\n"
    "s_mov_b32 s91, s89\n"
    "LNL80_%=:\n"
    "s_sub_u32 s91, s91, 1\n"
    "s_cmp_gt_i32 s91, 0\n"
    "s_cbranch_scc1 LNL80_%=\n"
    "s_branch LNR80_%=\n"
    "LND81_%=:\n"
    "s_mov_b32 s91, s89\n"
    "LNL81_%=:\n"
    "s_sub_u32 s91, s91, 1\n"
    "s_cmp_gt_i32 s91, 0\n"
    "s_cbranch_scc1 LNL81_%=\n"
    "s_branch LNR81_%=\n"
    "LND82_%=:\n"
    "s_mov_b32 s91, s89\n"
    "LNL82_%=:\n"
    "s_sub_u32 s91, s91, 1\n"
    "s_cmp_gt_i32 s91, 0\n"
    "s_cbranch_scc1 LNL82_%=\n"
    "s_branch LNR82_%=\n"
    "LND83_%=:\n"
    "s_mov_b32 s91, s89\n"
    "LNL83_%=:\n"
    "s_sub_u32 s91, s91, 1\n"
    "s_cmp_gt_i32 s91, 0\n"
    "s_cbranch_scc1 LNL83_%=\n"
    "s_branch LNR83_%=\n"
    "LNX5_%=:\n"
    "s_waitcnt lgkmcnt(6)\n"
    "v_mfma_f32_32x32x16_bf16 v[0:15], v[160:163], v[128:131], 0\n"
    "ds_read_b128 v[188:191], v247 offset:0\n"
    "v_mbcnt_lo_u32_b32 v224, -1, 0\n"
    "v_mbcnt_hi_u32_b32 v224, -1, v224\n"
    "s_lshl_b32 s62, s58, 12\n"
    "s_waitcnt lgkmcnt(6)\n"
    "v_mfma_f32_32x32x16_bf16 v[0:15], v[164:167], v[132:135], v[0:15]\n"
    "ds_read_b128 v[160:163], v240 offset:8192\n"
    "s_add_u32 s62, s62, 131072\n"
    "v_and_b32 v225, 7, v224\n"
    "v_lshrrev_b32 v226, 3, v224\n"
    "s_waitcnt lgkmcnt(6)\n"
    "v_mfma_f32_32x32x16_bf16 v[0:15], v[168:171], v[136:139], v[0:15]\n"
    "ds_read_b128 v[164:167], v241 offset:8192\n"
    "v_lshlrev_b32 v254, 4, v225\n"
    "v_lshl_or_b32 v254, v226, 8, v254\n"
    "s_add_u32 m0, s54, 65536\n"
    "s_add_u32 s55, s53, 0\n"
    "buffer_load_dwordx4 v255, s[44:47], s55 offen nt lds\n"
    "v_xor_b32 v249, v225, v226\n"
    "s_waitcnt lgkmcnt(6)\n"
    "v_mfma_f32_32x32x16_bf16 v[0:15], v[172:175], v[140:143], v[0:15]\n"
    "s_cbranch_vccnz LND80_%=\n"
    "LNR80_%=:\n"
    "ds_read_b128 v[168:171], v242 offset:8192\n"
    "v_lshlrev_b32 v249, 4, v249\n"
    "v_lshl_or_b32 v249, v226, 7, v249\n"
    "v_add_u32 v249, s62, v249\n"
    "s_waitcnt lgkmcnt(6)\n"
    "v_mfma_f32_32x32x16_bf16 v[0:15], v[176:179], v[144:147], v[0:15]\n"
    "s_sub_u32 s88, s84, s90\n"
    "s_sub_u32 s85, s86, s92\n"
    "s_mov_b32 s90, s84\n"
    "s_mov_b32 s92, s86\n"
    "s_mul_i32 s87, s85, 182\n"
    "s_mul_i32 s91, s88, 10\n"
    "s_cmp_ge_u32 s91, s87\n"
    "s_cselect_b32 s88, s88, 0\n"
    "s_mul_i32 s88, s88, 45\n"
    "s_mov_b32 s89, 0\n"
    "s_mul_i32 s87, s85, 737\n"
    "s_cmp_ge_u32 s88, s87\n"
    "s_addc_u32 s89, s89, 0\n"
    "s_mul_i32 s87, s85, 802\n"
    "s_cmp_ge_u32 s88, s87\n"
    "s_addc_u32 s89, s89, 0\n"
    "s_mul_i32 s87, s85, 867\n"
    "s_cmp_ge_u32 s88, s87\n"
    "s_addc_u32 s89, s89, 0\n"
    "s_mul_i32 s87, s85, 932\n"
    "s_cmp_ge_u32 s88, s87\n"
    "s_addc_u32 s89, s89, 0\n"
    "s_mul_i32 s87, s85, 997\n"
    "s_cmp_ge_u32 s88, s87\n"
    "s_addc_u32 s89, s89, 0\n"
    "s_mul_i32 s87, s85, 1062\n"
    "s_cmp_ge_u32 s88, s87\n"
    "s_addc_u32 s89, s89, 0\n"
    "s_cmp_lg_u32 s89, 0\n"
    "s_cselect_b64 vcc, -1, 0\n"
    "ds_read_b128 v[172:175], v243 offset:8192\n"
    "v_and_b32 v226, 31, v224\n"
    "v_lshrrev_b32 v224, 5, v224\n"
    "v_lshlrev_b32 v252, 7, v226\n"
    "s_waitcnt lgkmcnt(6)\n"
    "v_mfma_f32_32x32x16_bf16 v[0:15], v[180:183], v[148:151], v[0:15]\n"
    "ds_read_b128 v[176:179], v244 offset:8192\n"
    "v_lshl_or_b32 v252, v225, 4, v252\n"
    "v_lshl_or_b32 v252, v224, 3, v252\n"
    "v_add_u32 v252, s62, v252\n"
    "s_waitcnt lgkmcnt(6)\n"
    "v_mfma_f32_32x32x16_bf16 v[0:15], v[184:187], v[152:155], v[0:15]\n"
    "ds_read_b128 v[180:183], v245 offset:8192\n"
    "s_waitcnt lgkmcnt(6)\n"
    "v_mfma_f32_32x32x16_bf16 v[0:15], v[188:191], v[156:159], v[0:15]\n"
    "s_cbranch_vccnz LND81_%=\n"
    "LNR81_%=:\n"
    "ds_read_b128 v[184:187], v246 offset:8192\n"
    "v_xor_b32 v228, 0, v252\n"
    "ds_write_b64 v228, v[192:193] offset:0\n"
    "s_waitcnt lgkmcnt(7)\n"
    "v_mfma_f32_32x32x16_bf16 v[16:31], v[160:163], v[128:131], 0\n"
    "ds_read_b128 v[188:191], v247 offset:8192\n"
    "s_waitcnt lgkmcnt(7)\n"
    "v_mfma_f32_32x32x16_bf16 v[16:31], v[164:167], v[132:135], v[16:31]\n"
    "buffer_load_dwordx4 v255, s[44:47], s55 offen offset:1024 nt lds\n"
    "s_waitcnt vmcnt(6)\n"
    "s_barrier\n"
    "ds_read_b128 v[160:163], v240 offset:16384\n"
    "s_waitcnt lgkmcnt(7)\n"
    "v_mfma_f32_32x32x16_bf16 v[16:31], v[168:171], v[136:139], v[16:31]\n"
    "ds_read_b128 v[164:167], v241 offset:16384\n"
    "v_xor_b32 v229, 16, v252\n"
    "ds_write_b64 v229, v[194:195] offset:0\n"
    "v_max_f32 v232, v0, v1\n"
    "s_waitcnt lgkmcnt(8)\n"
    "v_mfma_f32_32x32x16_bf16 v[16:31], v[172:175], v[140:143], v[16:31]\n"
    "s_cbranch_vccnz LND82_%=\n"
    "LNR82_%=:\n"
    "ds_read_b128 v[168:171], v242 offset:16384\n"
    "v_max_f32 v233, v2, v3\n"
    "v_max3_f32 v232, v232, v4, v5\n"
    "s_add_u32 m0, s54, 81920\n"
    "s_add_u32 s55, s53, 16384\n"
    "buffer_load_dwordx4 v255, s[44:47], s55 offen nt lds\n"
    "v_max3_f32 v233, v233, v6, v7\n"
    "v_max3_f32 v232, v232, v8, v9\n"
    "s_waitcnt lgkmcnt(8)\n"
    "v_mfma_f32_32x32x16_bf16 v[16:31], v[176:179], v[144:147], v[16:31]\n"
    "s_memtime s[84:85]\n"
    "s_memrealtime s[86:87]\n"
    "ds_read_b128 v[172:175], v243 offset:16384\n"
    "v_max3_f32 v233, v233, v10, v11\n"
    "v_max3_f32 v232, v232, v12, v13\n"
    "v_max3_f32 v233, v233, v14, v15\n"
    "v_max_f32 v232, v232, v233\n"
    "s_waitcnt lgkmcnt(8)\n"
    "v_mfma_f32_32x32x16_bf16 v[16:31], v[180:183], v[148:151], v[16:31]\n"
    "ds_read_b128 v[176:179], v244 offset:16384\n"
    "v_xor_b32 v228, 32, v252\n"
    "ds_write_b64 v228, v[196:197] offset:0\n"
    "v_mov_b32 v233, v232\n"
    "s_waitcnt lgkmcnt(9)\n"
    "v_mfma_f32_32x32x16_bf16 v[16:31], v[184:187], v[152:155], v[16:31]\n"
    "ds_read_b128 v[180:183], v245 offset:16384\n"
    "v_permlane32_swap_b32 v232, v233\n"
    "v_max_f32 v232, v232, v233\n"
    "v_mul_f32 v248, 0xbe0293ee, v232\n"
    "v_mov_b32 v232, 0\n"
    "s_waitcnt lgkmcnt(8)\n"
    "v_mfma_f32_32x32x16_bf16 v[16:31], v[188:191], v[156:159], v[16:31]\n"
    "s_cbranch_vccnz LND83_%=\n"
    "LNR83_%=:\n"
    "ds_read_b128 v[184:187], v246 offset:16384\n"
    "v_mov_b32 v233, 0\n"
    "v_fma_f32 v0, v0, s56, v248\n"
    "v_fma_f32 v1, v1, s56, v248\n"
    "v_fma_f32 v2, v2, s56, v248\n"
    "s_branch LNX6_%=\n"
    "LND96_%=:\n"
    "s_mov_b32 s91, s89\n"
    "LNL96_%=:\n"
    "s_sub_u32 s91, s91, 1\n"
    "s_cmp_gt_i32 s91, 0\n"
    "s_cbranch_scc1 LNL96_%=\n"
    "s_branch LNR96_%=\n"
    "LND97_%=:\n"
    "s_mov_b32 s91, s89\n"
    "LNL97_%=:\n"
    "s_sub_u32 s91, s91, 1\n"
    "s_cmp_gt_i32 s91, 0\n"
    "s_cbranch_scc1 LNL97_%=\n"
    "s_branch LNR97_%=\n"
    "LND98_%=:\n"
    "s_mov_b32 s91, s89\n"
    "LNL98_%=:\n"
    "s_sub_u32 s91, s91, 1\n"
    "s_cmp_gt_i32 s91, 0\n"
    "s_cbranch_scc1 LNL98_%=\n"
    "s_branch LNR98_%=\n"
    "LND99_%=:\n"
    "s_mov_b32 s91, s89\n"
    "LNL99_%=:\n"
    "s_sub_u32 s91, s91, 1\n"
    "s_cmp_gt_i32 s91, 0\n"
    "s_cbranch_scc1 LNL99_%=\n"
    "s_branch LNR99_%=\n"
    "LNX6_%=:\n"
    "s_waitcnt lgkmcnt(8)\n"
    "v_mfma_f32_32x32x16_bf16 v[32:47], v[160:163], v[128:131], 0\n"
    "ds_read_b128 v[188:191], v247 offset:16384\n"
    "v_xor_b32 v229, 48, v252\n"
    "ds_write_b64 v229, v[198:199] offset:0\n"
    "v_fma_f32 v3, v3, s56, v248\n"
    "s_waitcnt lgkmcnt(9)\n"
    "v_mfma_f32_32x32x16_bf16 v[32:47], v[164:167], v[132:135], v[32:47]\n"
    "ds_read_b128 v[160:163], v240 offset:24576\n"
    "v_fma_f32 v4, v4, s56, v248\n"
    "v_fma_f32 v5, v5, s56, v248\n"
    "v_fma_f32 v6, v6, s56, v248\n"
    "v_fma_f32 v7, v7, s56, v248\n"
    "s_waitcnt lgkmcnt(8)\n"
    "v_mfma_f32_32x32x16_bf16 v[32:47], v[168:171], v[136:139], v[32:47]\n"
    "ds_read_b128 v[164:167], v241 offset:24576\n"
    "v_fma_f32 v8, v8, s56, v248\n"
    "v_fma_f32 v9, v9, s56, v248\n"
    "buffer_load_dwordx4 v255, s[44:47], s55 offen offset:1024 nt lds\n"
    "v_fma_f32 v10, v10, s56, v248\n"
    "v_fma_f32 v11, v11, s56, v248\n"
    "s_waitcnt lgkmcnt(8)\n"
    "v_mfma_f32_32x32x16_bf16 v[32:47], v[172:175], v[140:143], v[32:47]\n"
    "s_cbranch_vccnz LND96_%=\n"
    "LNR96_%=:\n"
    "ds_read_b128 v[168:171], v242 offset:24576\n"
    "v_xor_b32 v228, 64, v252\n"
    "ds_write_b64 v228, v[200:201] offset:0\n"
    "v_fma_f32 v12, v12, s56, v248\n"
    "s_waitcnt lgkmcnt(9)\n"
    "v_mfma_f32_32x32x16_bf16 v[32:47], v[176:179], v[144:147], v[32:47]\n"
    "ds_read_b128 v[172:175], v243 offset:24576\n"
    "v_fma_f32 v13, v13, s56, v248\n"
    "v_fma_f32 v14, v14, s56, v248\n"
    "v_fma_f32 v15, v15, s56, v248\n"
    "v_fma_f32 v16, v16, s56, v248\n"
    "s_waitcnt lgkmcnt(8)\n"
    "v_mfma_f32_32x32x16_bf16 v[32:47], v[180:183], v[148:151], v[32:47]\n"
    "ds_read_b128 v[176:179], v244 offset:24576\n"
    "v_fma_f32 v17, v17, s56, v248\n"
    "v_fma_f32 v18, v18, s56, v248\n"
    "v_fma_f32 v19, v19, s56, v248\n"
    "v_fma_f32 v20, v20, s56, v248\n"
    "s_waitcnt lgkmcnt(8)\n"
    "v_mfma_f32_32x32x16_bf16 v[32:47], v[184:187], v[152:155], v[32:47]\n"
    "ds_read_b128 v[180:183], v245 offset:24576\n"
    "v_xor_b32 v229, 80, v252\n"
    "ds_write_b64 v229, v[202:203] offset:0\n"
    "v_fma_f32 v21, v21, s56, v248\n"
    "s_waitcnt lgkmcnt(9)\n"
    "v_mfma_f32_32x32x16_bf16 v[32:47], v[188:191], v[156:159], v[32:47]\n"
    "s_cbranch_vccnz LND97_%=\n"
    "LNR97_%=:\n"
    "ds_read_b128 v[184:187], v246 offset:24576\n"
    "v_fma_f32 v22, v22, s56, v248\n"
    "v_fma_f32 v23, v23, s56, v248\n"
    "v_fma_f32 v24, v24, s56, v248\n"
    "v_fma_f32 v25, v25, s56, v248\n"
    "s_waitcnt lgkmcnt(8)\n"
    "v_mfma_f32_32x32x16_bf16 v[48:63], v[160:163], v[128:131], 0\n"
    "ds_read_b128 v[188:191], v247 offset:24576\n"
    "v_fma_f32 v26, v26, s56, v248\n"
    "v_fma_f32 v27, v27, s56, v248\n"
    "v_fma_f32 v28, v28, s56, v248\n"
    "v_fma_f32 v29, v29, s56, v248\n"
    "s_waitcnt lgkmcnt(8)\n"
    "v_mfma_f32_32x32x16_bf16 v[48:63], v[164:167], v[132:135], v[48:63]\n"
    "s_waitcnt vmcnt(6)\n"
    "s_barrier\n"
    "ds_read_b128 v[160:163], v240 offset:32768\n"
    "v_xor_b32 v228, 96, v252\n"
    "ds_write_b64 v228, v[204:205] offset:0\n"
    "v_fma_f32 v30, v30, s56, v248\n"
    "s_waitcnt lgkmcnt(9)\n"
    "v_mfma_f32_32x32x16_bf16 v[48:63], v[168:171], v[136:139], v[48:63]\n"
    "ds_read_b128 v[164:167], v241 offset:32768\n"
    "v_fma_f32 v31, v31, s56, v248\n"
    "v_fma_f32 v32, v32, s56, v248\n"
    "v_fma_f32 v33, v33, s56, v248\n"
    "v_fma_f32 v34, v34, s56, v248\n"
    "s_waitcnt lgkmcnt(8)\n"
    "v_mfma_f32_32x32x16_bf16 v[48:63], v[172:175], v[140:143], v[48:63]\n"
    "s_cbranch_vccnz LND98_%=\n"
    "LNR98_%=:\n"
    "ds_read_b128 v[168:171], v242 offset:32768\n"
    "v_fma_f32 v35, v35, s56, v248\n"
    "v_fma_f32 v36, v36, s56, v248\n"
    "s_add_u32 m0, s54, 98304\n"
    "s_add_u32 s55, s53, 32768\n"
    "buffer_load_dwordx4 v255, s[44:47], s55 offen nt lds\n"
    "v_fma_f32 v37, v37, s56, v248\n"
    "v_fma_f32 v38, v38, s56, v248\n"
    "s_waitcnt lgkmcnt(8)\n"
    "v_mfma_f32_32x32x16_bf16 v[48:63], v[176:179], v[144:147], v[48:63]\n"
    "s_memtime s[84:85]\n"
    "s_memrealtime s[86:87]\n"
    "ds_read_b128 v[172:175], v243 offset:32768\n"
    "v_xor_b32 v229, 112, v252\n"
    "ds_write_b64 v229, v[206:207] offset:0\n"
    "v_fma_f32 v39, v39, s56, v248\n"
    "s_waitcnt lgkmcnt(9)\n"
    "v_mfma_f32_32x32x16_bf16 v[48:63], v[180:183], v[148:151], v[48:63]\n"
    "ds_read_b128 v[176:179], v244 offset:32768\n"
    "v_fma_f32 v40, v40, s56, v248\n"
    "v_fma_f32 v41, v41, s56, v248\n"
    "v_fma_f32 v42, v42, s56, v248\n"
    "v_fma_f32 v43, v43, s56, v248\n"
    "s_waitcnt lgkmcnt(8)\n"
    "v_mfma_f32_32x32x16_bf16 v[48:63], v[184:187], v[152:155], v[48:63]\n"
    "ds_read_b128 v[180:183], v245 offset:32768\n"
    "v_fma_f32 v44, v44, s56, v248\n"
    "v_fma_f32 v45, v45, s56, v248\n"
    "v_fma_f32 v46, v46, s56, v248\n"
    "v_fma_f32 v47, v47, s56, v248\n"
    "s_waitcnt lgkmcnt(8)\n"
    "v_mfma_f32_32x32x16_bf16 v[48:63], v[188:191], v[156:159], v[48:63]\n"
    "s_cbranch_vccnz LND99_%=\n"
    "LNR99_%=:\n"
    "ds_read_b128 v[184:187], v246 offset:32768\n"
    "ds_read_b128 v[192:195], v249 offset:0\n"
    "v_exp_f32 v234, v0\n"
    "s_branch LNX7_%=\n"
    "LND112_%=:\n"
    "s_mov_b32 s91, s89\n"
    "LNL112_%=:\n"
    "s_sub_u32 s91, s91, 1\n"
    "s_cmp_gt_i32 s91, 0\n"
    "s_cbranch_scc1 LNL112_%=\n"
    "s_branch LNR112_%=\n"
    "LND113_%=:\n"
    "s_mov_b32 s91, s89\n"
    "LNL113_%=:\n"
    "s_sub_u32 s91, s91, 1\n"
    "s_cmp_gt_i32 s91, 0\n"
    "s_cbranch_scc1 LNL113_%=\n"
    "s_branch LNR113_%=\n"
    "LND114_%=:\n"
    "s_mov_b32 s91, s89\n"
    "LNL114_%=:\n"
    "s_sub_u32 s91, s91, 1\n"
    "s_cmp_gt_i32 s91, 0\n"
    "s_cbranch_scc1 LNL114_%=\n"
    "s_branch LNR114_%=\n"
    "LND115_%=:\n"
    "s_mov_b32 s91, s89\n"
    "LNL115_%=:\n"
    "s_sub_u32 s91, s91, 1\n"
    "s_cmp_gt_i32 s91, 0\n"
    "s_cbranch_scc1 LNL115_%=\n"
    "s_branch LNR115_%=\n"
    "LNX7_%=:\n"
    "s_waitcnt lgkmcnt(9)\n"
    "v_mfma_f32_32x32x16_bf16 v[64:79], v[160:163], v[128:131], 0\n"
    "s_sub_u32 s88, s84, s90\n"
    "s_sub_u32 s85, s86, s92\n"
    "s_mov_b32 s90, s84\n"
    "s_mov_b32 s92, s86\n"
    "s_mul_i32 s87, s85, 182\n"
    "s_mul_i32 s91, s88, 10\n"
    "s_cmp_ge_u32 s91, s87\n"
    "s_cselect_b32 s88, s88, 0\n"
    "s_mul_i32 s88, s88, 45\n"
    "s_mov_b32 s89, 0\n"
    "s_mul_i32 s87, s85, 737\n"
    "s_cmp_ge_u32 s88, s87\n"
    "s_addc_u32 s89, s89, 0\n"
    "s_mul_i32 s87, s85, 802\n"
    "s_cmp_ge_u32 s88, s87\n"
    "s_addc_u32 s89, s89, 0\n"
    "s_mul_i32 s87, s85, 867\n"
    "s_cmp_ge_u32 s88, s87\n"
    "s_addc_u32 s89, s89, 0\n"
    "s_mul_i32 s87, s85, 932\n"
    "s_cmp_ge_u32 s88, s87\n"
    "s_addc_u32 s89, s89, 0\n"
    "s_mul_i32 s87, s85, 997\n"
    "s_cmp_ge_u32 s88, s87\n"
    "s_addc_u32 s89, s89, 0\n"
    "s_mul_i32 s87, s85, 1062\n"
    "s_cmp_ge_u32 s88, s87\n"
    "s_addc_u32 s89, s89, 0\n"
    "s_cmp_lg_u32 s89, 0\n"
    "s_cselect_b64 vcc, -1, 0\n"
    "ds_read_b128 v[188:191], v247 offset:32768\n"
    "ds_read_b128 v[196:199], v249 offset:1024\n"
    "v_exp_f32 v235, v1\n"
    "s_nop 0\n"
    "v_cvt_pk_bf16_f32 v236, v234, v235\n"
    "s_waitcnt lgkmcnt(9)\n"
    "v_mfma_f32_32x32x16_bf16 v[64:79], v[164:167], v[132:135], v[64:79]\n"
    "ds_read_b128 v[160:163], v240 offset:40960\n"
    "ds_read_b128 v[200:203], v249 offset:2048\n"
    "v_mov_b32 v250, v234\n"
    "v_mov_b32 v251, v235\n"
    "s_waitcnt lgkmcnt(10)\n"
    "v_mfma_f32_32x32x16_bf16 v[64:79], v[168:171], v[136:139], v[64:79]\n"
    "ds_read_b128 v[164:167], v241 offset:40960\n"
    "ds_read_b128 v[204:207], v249 offset:3072\n"
    "v_fma_f32 v48, v48, s56, v248\n"
    "buffer_load_dwordx4 v255, s[44:47], s55 offen offset:1024 nt lds\n"
    "v_fma_f32 v49, v49, s56, v248\n"
    "v_fma_f32 v50, v50, s56, v248\n"
    "s_waitcnt lgkmcnt(11)\n"
    "v_mfma_f32_32x32x16_bf16 v[64:79], v[172:175], v[140:143], v[64:79]\n"
    "s_cbranch_vccnz LND112_%=\n"
    "LNR112_%=:\n"
    "ds_read_b128 v[168:171], v242 offset:40960\n"
    "v_xor_b32 v228, 0, v252\n"
    "ds_write_b64 v228, v[208:209] offset:0\n"
    "v_fma_f32 v51, v51, s56, v248\n"
    "s_waitcnt lgkmcnt(11)\n"
    "v_mfma_f32_32x32x16_bf16 v[64:79], v[176:179], v[144:147], v[64:79]\n"
    "ds_read_b128 v[172:175], v243 offset:40960\n"
    "v_fma_f32 v52, v52, s56, v248\n"
    "v_fma_f32 v53, v53, s56, v248\n"
    "v_fma_f32 v54, v54, s56, v248\n"
    "v_fma_f32 v55, v55, s56, v248\n"
    "s_waitcnt lgkmcnt(11)\n"
    "v_mfma_f32_32x32x16_bf16 v[64:79], v[180:183], v[148:151], v[64:79]\n"
    "ds_read_b128 v[176:179], v244 offset:40960\n"
    "v_fma_f32 v56, v56, s56, v248\n"
    "v_fma_f32 v57, v57, s56, v248\n"
    "v_fma_f32 v58, v58, s56, v248\n"
    "v_fma_f32 v59, v59, s56, v248\n"
    "s_waitcnt lgkmcnt(11)\n"
    "v_mfma_f32_32x32x16_bf16 v[64:79], v[184:187], v[152:155], v[64:79]\n"
    "ds_read_b128 v[180:183], v245 offset:40960\n"
    "v_xor_b32 v229, 16, v252\n"
    "ds_write_b64 v229, v[210:211] offset:0\n"
    "v_fma_f32 v60, v60, s56, v248\n"
    "s_waitcnt lgkmcnt(11)\n"
    "v_mfma_f32_32x32x16_bf16 v[64:79], v[188:191], v[156:159], v[64:79]\n"
    "s_cbranch_vccnz LND113_%=\n"
    "LNR113_%=:\n"
    "ds_read_b128 v[184:187], v246 offset:40960\n"
    "v_fma_f32 v61, v61, s56, v248\n"
    "v_fma_f32 v62, v62, s56, v248\n"
    "v_fma_f32 v63, v63, s56, v248\n"
    "s_waitcnt lgkmcnt(10)\n"
    "v_mfma_f32_32x32x16_bf16 v[80:95], v[160:163], v[128:131], 0\n"
    "ds_read_b128 v[188:191], v247 offset:40960\n"
    "v_exp_f32 v234, v2\n"
    "v_exp_f32 v235, v3\n"
    "s_waitcnt lgkmcnt(9)\n"
    "v_mfma_f32_32x32x16_bf16 v[80:95], v[164:167], v[132:135], v[80:95]\n"
    "s_waitcnt vmcnt(6)\n"
    "s_barrier\n"
    "ds_read_b128 v[160:163], v240 offset:49152\n"
    "v_xor_b32 v228, 32, v252\n"
    "ds_write_b64 v228, v[212:213] offset:0\n"
    "v_cvt_pk_bf16_f32 v237, v234, v235\n"
    "s_waitcnt lgkmcnt(9)\n"
    "v_mfma_f32_32x32x16_bf16 v[80:95], v[168:171], v[136:139], v[80:95]\n"
    "ds_read_b128 v[164:167], v241 offset:49152\n"
    "v_fma_f32 v64, v64, s56, v248\n"
    "v_fma_f32 v65, v65, s56, v248\n"
    "v_fma_f32 v66, v66, s56, v248\n"
    "v_fma_f32 v67, v67, s56, v248\n"
    "s_waitcnt lgkmcnt(8)\n"
    "v_mfma_f32_32x32x16_bf16 v[80:95], v[172:175], v[140:143], v[80:95]\n"
    "s_cbranch_vccnz LND114_%=\n"
    "LNR114_%=:\n"
    "ds_read_b128 v[168:171], v242 offset:49152\n"
    "v_fma_f32 v68, v68, s56, v248\n"
    "v_fma_f32 v69, v69, s56, v248\n"
    "s_add_u32 m0, s54, 114688\n"
    "s_add_u32 s55, s53, 49152\n"
    "buffer_load_dwordx4 v255, s[44:47], s55 offen nt lds\n"
    "v_fma_f32 v70, v70, s56, v248\n"
    "v_fma_f32 v71, v71, s56, v248\n"
    "s_waitcnt lgkmcnt(8)\n"
    "v_mfma_f32_32x32x16_bf16 v[80:95], v[176:179], v[144:147], v[80:95]\n"
    "s_memtime s[84:85]\n"
    "s_memrealtime s[86:87]\n"
    "ds_read_b128 v[172:175], v243 offset:49152\n"
    "v_xor_b32 v229, 48, v252\n"
    "ds_write_b64 v229, v[214:215] offset:0\n"
    "v_fma_f32 v72, v72, s56, v248\n"
    "s_waitcnt lgkmcnt(9)\n"
    "v_mfma_f32_32x32x16_bf16 v[80:95], v[180:183], v[148:151], v[80:95]\n"
    "ds_read_b128 v[176:179], v244 offset:49152\n"
    "v_fma_f32 v73, v73, s56, v248\n"
    "v_fma_f32 v74, v74, s56, v248\n"
    "v_fma_f32 v75, v75, s56, v248\n"
    "v_fma_f32 v76, v76, s56, v248\n"
    "s_waitcnt lgkmcnt(8)\n"
    "v_mfma_f32_32x32x16_bf16 v[80:95], v[184:187], v[152:155], v[80:95]\n"
    "ds_read_b128 v[180:183], v245 offset:49152\n"
    "v_fma_f32 v77, v77, s56, v248\n"
    "v_fma_f32 v78, v78, s56, v248\n"
    "v_fma_f32 v79, v79, s56, v248\n"
    "v_add_f32 v250, v250, v234\n"
    "s_waitcnt lgkmcnt(8)\n"
    "v_mfma_f32_32x32x16_bf16 v[80:95], v[188:191], v[156:159], v[80:95]\n"
    "s_cbranch_vccnz LND115_%=\n"
    "LNR115_%=:\n"
    "ds_read_b128 v[184:187], v246 offset:49152\n"
    "v_xor_b32 v228, 64, v252\n"
    "ds_write_b64 v228, v[216:217] offset:0\n"
    "v_add_f32 v251, v251, v235\n"
    "s_branch LNX8_%=\n"
    "LND128_%=:\n"
    "s_mov_b32 s91, s89\n"
    "LNL128_%=:\n"
    "s_sub_u32 s91, s91, 1\n"
    "s_cmp_gt_i32 s91, 0\n"
    "s_cbranch_scc1 LNL128_%=\n"
    "s_branch LNR128_%=\n"
    "LND129_%=:\n"
    "s_mov_b32 s91, s89\n"
    "LNL129_%=:\n"
    "s_sub_u32 s91, s91, 1\n"
    "s_cmp_gt_i32 s91, 0\n"
    "s_cbranch_scc1 LNL129_%=\n"
    "s_branch LNR129_%=\n"
    "LND130_%=:\n"
    "s_mov_b32 s91, s89\n"
    "LNL130_%=:\n"
    "s_sub_u32 s91, s91, 1\n"
    "s_cmp_gt_i32 s91, 0\n"
    "s_cbranch_scc1 LNL130_%=\n"
    "s_branch LNR130_%=\n"
    "LND131_%=:\n"
    "s_mov_b32 s91, s89\n"
    "LNL131_%=:\n"
    "s_sub_u32 s91, s91, 1\n"
    "s_cmp_gt_i32 s91, 0\n"
    "s_cbranch_scc1 LNL131_%=\n"
    "s_branch LNR131_%=\n"
    "LNX8_%=:\n"
    "s_waitcnt lgkmcnt(9)\n"
    "v_mfma_f32_32x32x16_bf16 v[96:111], v[160:163], v[128:131], 0\n"
    "s_sub_u32 s88, s84, s90\n"
    "s_sub_u32 s85, s86, s92\n"
    "s_mov_b32 s90, s84\n"
    "s_mov_b32 s92, s86\n"
    "s_mul_i32 s87, s85, 182\n"
    "s_mul_i32 s91, s88, 10\n"
    "s_cmp_ge_u32 s91, s87\n"
    "s_cselect_b32 s88, s88, 0\n"
    "s_mul_i32 s88, s88, 45\n"
    "s_mov_b32 s89, 0\n"
    "s_mul_i32 s87, s85, 737\n"
    "s_cmp_ge_u32 s88, s87\n"
    "s_addc_u32 s89, s89, 0\n"
    "s_mul_i32 s87, s85, 802\n"
    "s_cmp_ge_u32 s88, s87\n"
    "s_addc_u32 s89, s89, 0\n"
    "s_mul_i32 s87, s85, 867\n"
    "s_cmp_ge_u32 s88, s87\n"
    "s_addc_u32 s89, s89, 0\n"
    "s_mul_i32 s87, s85, 932\n"
    "s_cmp_ge_u32 s88, s87\n"
    "s_addc_u32 s89, s89, 0\n"
    "s_mul_i32 s87, s85, 997\n"
    "s_cmp_ge_u32 s88, s87\n"
    "s_addc_u32 s89, s89, 0\n"
    "s_mul_i32 s87, s85, 1062\n"
    "s_cmp_ge_u32 s88, s87\n"
    "s_addc_u32 s89, s89, 0\n"
    "s_cmp_lg_u32 s89, 0\n"
    "s_cselect_b64 vcc, -1, 0\n"
    "ds_read_b128 v[188:191], v247 offset:49152\n"
    "v_exp_f32 v234, v4\n"
    "v_exp_f32 v235, v5\n"
    "s_waitcnt lgkmcnt(8)\n"
    "v_mfma_f32_32x32x16_bf16 v[96:111], v[164:167], v[132:135], v[96:111]\n"
    "ds_read_b128 v[160:163], v240 offset:57344\n"
    "v_cvt_pk_bf16_f32 v238, v234, v235\n"
    "v_add_f32 v250, v250, v234\n"
    "v_add_f32 v251, v251, v235\n"
    "s_waitcnt lgkmcnt(8)\n"
    "v_mfma_f32_32x32x16_bf16 v[96:111], v[168:171], v[136:139], v[96:111]\n"
    "ds_read_b128 v[164:167], v241 offset:57344\n"
    "v_xor_b32 v229, 80, v252\n"
    "ds_write_b64 v229, v[218:219] offset:0\n"
    "buffer_load_dwordx4 v255, s[44:47], s55 offen offset:1024 nt lds\n"
    "v_fma_f32 v80, v80, s56, v248\n"
    "s_waitcnt lgkmcnt(9)\n"
    "v_mfma_f32_32x32x16_bf16 v[96:111], v[172:175], v[140:143], v[96:111]\n"
    "s_cbranch_vccnz LND128_%=\n"
    "LNR128_%=:\n"
    "ds_read_b128 v[168:171], v242 offset:57344\n"
    "v_fma_f32 v81, v81, s56, v248\n"
    "v_fma_f32 v82, v82, s56, v248\n"
    "v_fma_f32 v83, v83, s56, v248\n"
    "v_fma_f32 v84, v84, s56, v248\n"
    "s_waitcnt lgkmcnt(8)\n"
    "v_mfma_f32_32x32x16_bf16 v[96:111], v[176:179], v[144:147], v[96:111]\n"
    "ds_read_b128 v[172:175], v243 offset:57344\n"
    "v_fma_f32 v85, v85, s56, v248\n"
    "v_fma_f32 v86, v86, s56, v248\n"
    "v_fma_f32 v87, v87, s56, v248\n"
    "v_fma_f32 v88, v88, s56, v248\n"
    "s_waitcnt lgkmcnt(8)\n"
    "v_mfma_f32_32x32x16_bf16 v[96:111], v[180:183], v[148:151], v[96:111]\n"
    "ds_read_b128 v[176:179], v244 offset:57344\n"
    "v_xor_b32 v228, 96, v252\n"
    "ds_write_b64 v228, v[220:221] offset:0\n"
    "v_fma_f32 v89, v89, s56, v248\n"
    "s_waitcnt lgkmcnt(9)\n"
    "v_mfma_f32_32x32x16_bf16 v[96:111], v[184:187], v[152:155], v[96:111]\n"
    "ds_read_b128 v[180:183], v245 offset:57344\n"
    "v_fma_f32 v90, v90, s56, v248\n"
    "v_fma_f32 v91, v91, s56, v248\n"
    "v_fma_f32 v92, v92, s56, v248\n"
    "v_fma_f32 v93, v93, s56, v248\n"
    "s_waitcnt lgkmcnt(8)\n"
    "v_mfma_f32_32x32x16_bf16 v[96:111], v[188:191], v[156:159], v[96:111]\n"
    "s_cbranch_vccnz LND129_%=\n"
    "LNR129_%=:\n"
    "ds_read_b128 v[184:187], v246 offset:57344\n"
    "v_fma_f32 v94, v94, s56, v248\n"
    "v_fma_f32 v95, v95, s56, v248\n"
    "v_exp_f32 v234, v6\n"
    "s_waitcnt lgkmcnt(8)\n"
    "v_mfma_f32_32x32x16_bf16 v[112:127], v[160:163], v[128:131], 0\n"
    "ds_read_b128 v[188:191], v247 offset:57344\n"
    "v_xor_b32 v229, 112, v252\n"
    "ds_write_b64 v229, v[222:223] offset:0\n"
    "s_waitcnt lgkmcnt(9)\n"
    "v_mfma_f32_32x32x16_bf16 v[112:127], v[164:167], v[132:135], v[112:127]\n"
    "v_exp_f32 v235, v7\n"
    "s_nop 0\n"
    "v_cvt_pk_bf16_f32 v239, v234, v235\n"
    "v_add_f32 v250, v250, v234\n"
    "s_waitcnt lgkmcnt(7)\n"
    "v_mfma_f32_32x32x16_bf16 v[112:127], v[168:171], v[136:139], v[112:127]\n"
    "v_fma_f32 v96, v96, s56, v248\n"
    "v_fma_f32 v97, v97, s56, v248\n"
    "v_fma_f32 v98, v98, s56, v248\n"
    "v_fma_f32 v99, v99, s56, v248\n"
    "s_waitcnt lgkmcnt(6)\n"
    "v_mfma_f32_32x32x16_bf16 v[112:127], v[172:175], v[140:143], v[112:127]\n"
    "s_cbranch_vccnz LND130_%=\n"
    "LNR130_%=:\n"
    "v_fma_f32 v100, v100, s56, v248\n"
    "v_fma_f32 v101, v101, s56, v248\n"
    "v_fma_f32 v102, v102, s56, v248\n"
    "v_fma_f32 v103, v103, s56, v248\n"
    "s_waitcnt lgkmcnt(5)\n"
    "v_mfma_f32_32x32x16_bf16 v[112:127], v[176:179], v[144:147], v[112:127]\n"
    "s_memtime s[84:85]\n"
    "s_memrealtime s[86:87]\n"
    "v_fma_f32 v104, v104, s56, v248\n"
    "v_fma_f32 v105, v105, s56, v248\n"
    "v_fma_f32 v106, v106, s56, v248\n"
    "v_fma_f32 v107, v107, s56, v248\n"
    "s_waitcnt lgkmcnt(3)\n"
    "v_mfma_f32_32x32x16_bf16 v[112:127], v[180:183], v[148:151], v[112:127]\n"
    "v_fma_f32 v108, v108, s56, v248\n"
    "v_fma_f32 v109, v109, s56, v248\n"
    "v_fma_f32 v110, v110, s56, v248\n"
    "v_fma_f32 v111, v111, s56, v248\n"
    "s_waitcnt lgkmcnt(2)\n"
    "v_mfma_f32_32x32x16_bf16 v[112:127], v[184:187], v[152:155], v[112:127]\n"
    "v_add_f32 v251, v251, v235\n"
    "v_exp_f32 v234, v8\n"
    "s_waitcnt lgkmcnt(1)\n"
    "v_mfma_f32_32x32x16_bf16 v[112:127], v[188:191], v[156:159], v[112:127]\n"
    "s_cbranch_vccnz LND131_%=\n"
    "LNR131_%=:\n"
    "v_exp_f32 v235, v9\n"
    "v_xor_b32 v244, 65536, v253\n"
    "v_xor_b32 v245, 65600, v253\n"
    "v_xor_b32 v246, 65664, v253\n"
    "v_xor_b32 v247, 65728, v253\n"
    "s_waitcnt vmcnt(6)\n"
    "s_barrier\n"
    "ds_read_b64_tr_b16 v[208:209], v244 offset:0\n"
    "ds_read_b64_tr_b16 v[210:211], v244 offset:2048\n"
    "ds_read_b64_tr_b16 v[212:213], v245 offset:0\n"
    "ds_read_b64_tr_b16 v[214:215], v245 offset:2048\n"
    "ds_read_b64_tr_b16 v[216:217], v246 offset:0\n"
    "ds_read_b64_tr_b16 v[218:219], v246 offset:2048\n"
    "ds_read_b64_tr_b16 v[220:221], v247 offset:0\n"
    "ds_read_b64_tr_b16 v[222:223], v247 offset:2048\n"
    "ds_read_b64_tr_b16 v[224:225], v244 offset:4096\n"
    "ds_read_b64_tr_b16 v[226:227], v244 offset:6144\n"
    "ds_read_b64_tr_b16 v[228:229], v245 offset:4096\n"
    "ds_read_b64_tr_b16 v[230:231], v245 offset:6144\n"
    "s_branch LNX9_%=\n"
    "LND144_%=:\n"
    "s_mov_b32 s91, s89\n"
    "LNL144_%=:\n"
    "s_sub_u32 s91, s91, 1\n"
    "s_cmp_gt_i32 s91, 0\n"
    "s_cbranch_scc1 LNL144_%=\n"
    "s_branch LNR144_%=\n"
    "LND145_%=:\n"
    "s_mov_b32 s91, s89\n"
    "LNL145_%=:\n"
    "s_sub_u32 s91, s91, 1\n"
    "s_cmp_gt_i32 s91, 0\n"
    "s_cbranch_scc1 LNL145_%=\n"
    "s_branch LNR145_%=\n"
    "LND146_%=:\n"
    "s_mov_b32 s91, s89\n"
    "LNL146_%=:\n"
    "s_sub_u32 s91, s91, 1\n"
    "s_cmp_gt_i32 s91, 0\n"
    "s_cbranch_scc1 LNL146_%=\n"
    "s_branch LNR146_%=\n"
    "LND147_%=:\n"
    "s_mov_b32 s91, s89\n"
    "LNL147_%=:\n"
    "s_sub_u32 s91, s91, 1\n"
    "s_cmp_gt_i32 s91, 0\n"
    "s_cbranch_scc1 LNL147_%=\n"
    "s_branch LNR147_%=\n"
    "LNX9_%=:\n"
    "s_waitcnt lgkmcnt(10)\n"
    "v_mfma_f32_32x32x16_bf16 v[128:143], v[208:211], v[236:239], 0\n"
    "s_sub_u32 s88, s84, s90\n"
    "s_sub_u32 s85, s86, s92\n"
    "s_mov_b32 s90, s84\n"
    "s_mov_b32 s92, s86\n"
    "s_mul_i32 s87, s85, 182\n"
    "s_mul_i32 s91, s88, 10\n"
    "s_cmp_ge_u32 s91, s87\n"
    "s_cselect_b32 s88, s88, 0\n"
    "s_mul_i32 s88, s88, 48\n"
    "s_mov_b32 s89, 0\n"
    "s_mul_i32 s87, s85, 847\n"
    "s_cmp_ge_u32 s88, s87\n"
    "s_addc_u32 s89, s89, 0\n"
    "s_mul_i32 s87, s85, 912\n"
    "s_cmp_ge_u32 s88, s87\n"
    "s_addc_u32 s89, s89, 0\n"
    "s_mul_i32 s87, s85, 977\n"
    "s_cmp_ge_u32 s88, s87\n"
    "s_addc_u32 s89, s89, 0\n"
    "s_mul_i32 s87, s85, 1042\n"
    "s_cmp_ge_u32 s88, s87\n"
    "s_addc_u32 s89, s89, 0\n"
    "s_mul_i32 s87, s85, 1107\n"
    "s_cmp_ge_u32 s88, s87\n"
    "s_addc_u32 s89, s89, 0\n"
    "s_mul_i32 s87, s85, 1172\n"
    "s_cmp_ge_u32 s88, s87\n"
    "s_addc_u32 s89, s89, 0\n"
    "s_cmp_lg_u32 s89, 0\n"
    "s_cselect_b64 vcc, -1, 0\n"
    "v_cvt_pk_bf16_f32 v240, v234, v235\n"
    "v_add_f32 v250, v250, v234\n"
    "v_add_f32 v251, v251, v235\n"
    "ds_read_b64_tr_b16 v[232:233], v246 offset:4096\n"
    "ds_read_b64_tr_b16 v[234:235], v246 offset:6144\n"
    "v_exp_f32 v10, v10\n"
    "v_exp_f32 v11, v11\n"
    "s_waitcnt lgkmcnt(10)\n"
    "v_mfma_f32_32x32x16_bf16 v[144:159], v[212:215], v[236:239], 0\n"
    "ds_read_b64_tr_b16 v[208:209], v247 offset:4096\n"
    "ds_read_b64_tr_b16 v[210:211], v247 offset:6144\n"
    "v_exp_f32 v12, v12\n"
    "v_exp_f32 v13, v13\n"
    "v_cvt_pk_bf16_f32 v241, v10, v11\n"
    "v_add_f32 v250, v250, v10\n"
    "v_add_f32 v251, v251, v11\n"
    "s_waitcnt lgkmcnt(10)\n"
    "v_mfma_f32_32x32x16_bf16 v[160:175], v[216:219], v[236:239], 0\n"
    "ds_read_b64_tr_b16 v[212:213], v244 offset:8192\n"
    "ds_read_b64_tr_b16 v[214:215], v244 offset:10240\n"
    "v_exp_f32 v14, v14\n"
    "v_exp_f32 v15, v15\n"
    "v_cvt_pk_bf16_f32 v242, v12, v13\n"
    "v_add_f32 v250, v250, v12\n"
    "v_add_f32 v251, v251, v13\n"
    "v_fma_f32 v112, v112, s56, v248\n"
    "s_waitcnt lgkmcnt(10)\n"
    "v_mfma_f32_32x32x16_bf16 v[176:191], v[220:223], v[236:239], 0\n"
    "s_cbranch_vccnz LND144_%=\n"
    "LNR144_%=:\n"
    "ds_read_b64_tr_b16 v[216:217], v245 offset:8192\n"
    "ds_read_b64_tr_b16 v[218:219], v245 offset:10240\n"
    "v_exp_f32 v16, v16\n"
    "v_exp_f32 v17, v17\n"
    "v_cvt_pk_bf16_f32 v243, v14, v15\n"
    "v_add_f32 v250, v250, v14\n"
    "v_add_f32 v251, v251, v15\n"
    "v_fma_f32 v113, v113, s56, v248\n"
    "s_waitcnt lgkmcnt(10)\n"
    "v_mfma_f32_32x32x16_bf16 v[128:143], v[224:227], v[240:243], v[128:143]\n"
    "ds_read_b64_tr_b16 v[220:221], v246 offset:8192\n"
    "ds_read_b64_tr_b16 v[222:223], v246 offset:10240\n"
    "v_exp_f32 v18, v18\n"
    "v_exp_f32 v19, v19\n"
    "v_cvt_pk_bf16_f32 v236, v16, v17\n"
    "v_add_f32 v250, v250, v16\n"
    "v_add_f32 v251, v251, v17\n"
    "v_fma_f32 v114, v114, s56, v248\n"
    "s_waitcnt lgkmcnt(10)\n"
    "v_mfma_f32_32x32x16_bf16 v[144:159], v[228:231], v[240:243], v[144:159]\n"
    "ds_read_b64_tr_b16 v[224:225], v247 offset:8192\n"
    "ds_read_b64_tr_b16 v[226:227], v247 offset:10240\n"
    "v_exp_f32 v20, v20\n"
    "v_exp_f32 v21, v21\n"
    "v_cvt_pk_bf16_f32 v237, v18, v19\n"
    "v_add_f32 v250, v250, v18\n"
    "v_add_f32 v251, v251, v19\n"
    "v_fma_f32 v115, v115, s56, v248\n"
    "s_waitcnt lgkmcnt(10)\n"
    "v_mfma_f32_32x32x16_bf16 v[160:175], v[232:235], v[240:243], v[160:175]\n"
    "ds_read_b64_tr_b16 v[228:229], v244 offset:12288\n"
    "ds_read_b64_tr_b16 v[230:231], v244 offset:14336\n"
    "v_exp_f32 v22, v22\n"
    "v_exp_f32 v23, v23\n"
    "v_cvt_pk_bf16_f32 v238, v20, v21\n"
    "v_add_f32 v250, v250, v20\n"
    "v_add_f32 v251, v251, v21\n"
    "v_fma_f32 v116, v116, s56, v248\n"
    "s_waitcnt lgkmcnt(10)\n"
    "v_mfma_f32_32x32x16_bf16 v[176:191], v[208:211], v[240:243], v[176:191]\n"
    "s_cbranch_vccnz LND145_%=\n"
    "LNR145_%=:\n"
    "ds_read_b64_tr_b16 v[232:233], v245 offset:12288\n"
    "ds_read_b64_tr_b16 v[234:235], v245 offset:14336\n"
    "v_exp_f32 v24, v24\n"
    "v_exp_f32 v25, v25\n"
    "v_cvt_pk_bf16_f32 v239, v22, v23\n"
    "v_add_f32 v250, v250, v22\n"
    "v_add_f32 v251, v251, v23\n"
    "v_fma_f32 v117, v117, s56, v248\n"
    "s_waitcnt lgkmcnt(10)\n"
    "v_mfma_f32_32x32x16_bf16 v[128:143], v[212:215], v[236:239], v[128:143]\n"
    "ds_read_b64_tr_b16 v[208:209], v246 offset:12288\n"
    "ds_read_b64_tr_b16 v[210:211], v246 offset:14336\n"
    "v_exp_f32 v26, v26\n"
    "v_exp_f32 v27, v27\n"
    "v_cvt_pk_bf16_f32 v240, v24, v25\n"
    "v_add_f32 v250, v250, v24\n"
    "v_add_f32 v251, v251, v25\n"
    "v_fma_f32 v118, v118, s56, v248\n"
    "s_waitcnt lgkmcnt(10)\n"
    "v_mfma_f32_32x32x16_bf16 v[144:159], v[216:219], v[236:239], v[144:159]\n"
    "ds_read_b64_tr_b16 v[212:213], v247 offset:12288\n"
    "ds_read_b64_tr_b16 v[214:215], v247 offset:14336\n"
    "v_exp_f32 v28, v28\n"
    "v_exp_f32 v29, v29\n"
    "v_cvt_pk_bf16_f32 v241, v26, v27\n"
    "v_add_f32 v250, v250, v26\n"
    "v_add_f32 v251, v251, v27\n"
    "v_fma_f32 v119, v119, s56, v248\n"
    "s_waitcnt lgkmcnt(10)\n"
    "v_mfma_f32_32x32x16_bf16 v[160:175], v[220:223], v[236:239], v[160:175]\n"
    "s_waitcnt vmcnt(4)\n"
    "s_barrier\n"
    "ds_read_b64_tr_b16 v[216:217], v244 offset:16384\n"
    "ds_read_b64_tr_b16 v[218:219], v244 offset:18432\n"
    "v_exp_f32 v30, v30\n"
    "v_exp_f32 v31, v31\n"
    "v_cvt_pk_bf16_f32 v242, v28, v29\n"
    "v_add_f32 v250, v250, v28\n"
    "v_add_f32 v251, v251, v29\n"
    "v_fma_f32 v120, v120, s56, v248\n"
    "s_waitcnt lgkmcnt(10)\n"
    "v_mfma_f32_32x32x16_bf16 v[176:191], v[224:227], v[236:239], v[176:191]\n"
    "s_cbranch_vccnz LND146_%=\n"
    "LNR146_%=:\n"
    "ds_read_b64_tr_b16 v[220:221], v245 offset:16384\n"
    "ds_read_b64_tr_b16 v[222:223], v245 offset:18432\n"
    "v_exp_f32 v32, v32\n"
    "v_exp_f32 v33, v33\n"
    "v_cvt_pk_bf16_f32 v243, v30, v31\n"
    "v_add_f32 v250, v250, v30\n"
    "v_add_f32 v251, v251, v31\n"
    "v_fma_f32 v121, v121, s56, v248\n"
    "s_waitcnt lgkmcnt(10)\n"
    "v_mfma_f32_32x32x16_bf16 v[128:143], v[228:231], v[240:243], v[128:143]\n"
    "s_memtime s[84:85]\n"
    "s_memrealtime s[86:87]\n"
    "ds_read_b64_tr_b16 v[224:225], v246 offset:16384\n"
    "ds_read_b64_tr_b16 v[226:227], v246 offset:18432\n"
    "v_exp_f32 v34, v34\n"
    "v_exp_f32 v35, v35\n"
    "v_cvt_pk_bf16_f32 v236, v32, v33\n"
    "v_add_f32 v250, v250, v32\n"
    "v_add_f32 v251, v251, v33\n"
    "v_fma_f32 v122, v122, s56, v248\n"
    "s_waitcnt lgkmcnt(10)\n"
    "v_mfma_f32_32x32x16_bf16 v[144:159], v[232:235], v[240:243], v[144:159]\n"
    "ds_read_b64_tr_b16 v[228:229], v247 offset:16384\n"
    "ds_read_b64_tr_b16 v[230:231], v247 offset:18432\n"
    "v_exp_f32 v36, v36\n"
    "v_exp_f32 v37, v37\n"
    "v_cvt_pk_bf16_f32 v237, v34, v35\n"
    "v_add_f32 v250, v250, v34\n"
    "v_add_f32 v251, v251, v35\n"
    "v_fma_f32 v123, v123, s56, v248\n"
    "s_waitcnt lgkmcnt(10)\n"
    "v_mfma_f32_32x32x16_bf16 v[160:175], v[208:211], v[240:243], v[160:175]\n"
    "ds_read_b64_tr_b16 v[232:233], v244 offset:20480\n"
    "ds_read_b64_tr_b16 v[234:235], v244 offset:22528\n"
    "v_exp_f32 v38, v38\n"
    "v_exp_f32 v39, v39\n"
    "v_cvt_pk_bf16_f32 v238, v36, v37\n"
    "v_add_f32 v250, v250, v36\n"
    "v_add_f32 v251, v251, v37\n"
    "v_fma_f32 v124, v124, s56, v248\n"
    "s_waitcnt lgkmcnt(10)\n"
    "v_mfma_f32_32x32x16_bf16 v[176:191], v[212:215], v[240:243], v[176:191]\n"
    "s_cbranch_vccnz LND147_%=\n"
    "LNR147_%=:\n"
    "ds_read_b64_tr_b16 v[208:209], v245 offset:20480\n"
    "ds_read_b64_tr_b16 v[210:211], v245 offset:22528\n"
    "v_exp_f32 v40, v40\n"
    "v_exp_f32 v41, v41\n"
    "v_cvt_pk_bf16_f32 v239, v38, v39\n"
    "v_add_f32 v250, v250, v38\n"
    "v_add_f32 v251, v251, v39\n"
    "v_fma_f32 v125, v125, s56, v248\n"
    "s_branch LNX10_%=\n"
    "LND160_%=:\n"
    "s_mov_b32 s91, s89\n"
    "LNL160_%=:\n"
    "s_sub_u32 s91, s91, 1\n"
    "s_cmp_gt_i32 s91, 0\n"
    "s_cbranch_scc1 LNL160_%=\n"
    "s_branch LNR160_%=\n"
    "LND161_%=:\n"
    "s_mov_b32 s91, s89\n"
    "LNL161_%=:\n"
    "s_sub_u32 s91, s91, 1\n"
    "s_cmp_gt_i32 s91, 0\n"
    "s_cbranch_scc1 LNL161_%=\n"
    "s_branch LNR161_%=\n"
    "LND162_%=:\n"
    "s_mov_b32 s91, s89\n"
    "LNL162_%=:\n"
    "s_sub_u32 s91, s91, 1\n"
    "s_cmp_gt_i32 s91, 0\n"
    "s_cbranch_scc1 LNL162_%=\n"
    "s_branch LNR162_%=\n"
    "LND163_%=:\n"
    "s_mov_b32 s91, s89\n"
    "LNL163_%=:\n"
    "s_sub_u32 s91, s91, 1\n"
    "s_cmp_gt_i32 s91, 0\n"
    "s_cbranch_scc1 LNL163_%=\n"
    "s_branch LNR163_%=\n"
    "LNX10_%=:\n"
    "s_waitcnt lgkmcnt(10)\n"
    "v_mfma_f32_32x32x16_bf16 v[128:143], v[216:219], v[236:239], v[128:143]\n"
    "s_sub_u32 s88, s84, s90\n"
    "s_sub_u32 s85, s86, s92\n"
    "s_mov_b32 s90, s84\n"
    "s_mov_b32 s92, s86\n"
    "s_mul_i32 s87, s85, 182\n"
    "s_mul_i32 s91, s88, 10\n"
    "s_cmp_ge_u32 s91, s87\n"
    "s_cselect_b32 s88, s88, 0\n"
    "s_mul_i32 s88, s88, 48\n"
    "s_mov_b32 s89, 0\n"
    "s_mul_i32 s87, s85, 847\n"
    "s_cmp_ge_u32 s88, s87\n"
    "s_addc_u32 s89, s89, 0\n"
    "s_mul_i32 s87, s85, 912\n"
    "s_cmp_ge_u32 s88, s87\n"
    "s_addc_u32 s89, s89, 0\n"
    "s_mul_i32 s87, s85, 977\n"
    "s_cmp_ge_u32 s88, s87\n"
    "s_addc_u32 s89, s89, 0\n"
    "s_mul_i32 s87, s85, 1042\n"
    "s_cmp_ge_u32 s88, s87\n"
    "s_addc_u32 s89, s89, 0\n"
    "s_mul_i32 s87, s85, 1107\n"
    "s_cmp_ge_u32 s88, s87\n"
    "s_addc_u32 s89, s89, 0\n"
    "s_mul_i32 s87, s85, 1172\n"
    "s_cmp_ge_u32 s88, s87\n"
    "s_addc_u32 s89, s89, 0\n"
    "s_cmp_lg_u32 s89, 0\n"
    "s_cselect_b64 vcc, -1, 0\n"
    "ds_read_b64_tr_b16 v[212:213], v246 offset:20480\n"
    "ds_read_b64_tr_b16 v[214:215], v246 offset:22528\n"
    "v_exp_f32 v42, v42\n"
    "v_exp_f32 v43, v43\n"
    "v_cvt_pk_bf16_f32 v240, v40, v41\n"
    "v_add_f32 v250, v250, v40\n"
    "v_add_f32 v251, v251, v41\n"
    "v_fma_f32 v126, v126, s56, v248\n"
    "s_waitcnt lgkmcnt(10)\n"
    "v_mfma_f32_32x32x16_bf16 v[144:159], v[220:223], v[236:239], v[144:159]\n"
    "ds_read_b64_tr_b16 v[216:217], v247 offset:20480\n"
    "ds_read_b64_tr_b16 v[218:219], v247 offset:22528\n"
    "v_exp_f32 v44, v44\n"
    "v_exp_f32 v45, v45\n"
    "v_cvt_pk_bf16_f32 v241, v42, v43\n"
    "v_add_f32 v250, v250, v42\n"
    "v_add_f32 v251, v251, v43\n"
    "v_fma_f32 v127, v127, s56, v248\n"
    "s_waitcnt lgkmcnt(10)\n"
    "v_mfma_f32_32x32x16_bf16 v[160:175], v[224:227], v[236:239], v[160:175]\n"
    "ds_read_b64_tr_b16 v[220:221], v244 offset:24576\n"
    "ds_read_b64_tr_b16 v[222:223], v244 offset:26624\n"
    "v_exp_f32 v46, v46\n"
    "v_exp_f32 v47, v47\n"
    "v_cvt_pk_bf16_f32 v242, v44, v45\n"
    "v_add_f32 v250, v250, v44\n"
    "v_add_f32 v251, v251, v45\n"
    "ds_read_b128 v[16:19], v249 offset:0\n"
    "ds_read_b128 v[20:23], v249 offset:1024\n"
    "ds_read_b128 v[24:27], v249 offset:2048\n"
    "ds_read_b128 v[28:31], v249 offset:3072\n"
    "s_waitcnt lgkmcnt(14)\n"
    "v_mfma_f32_32x32x16_bf16 v[176:191], v[228:231], v[236:239], v[176:191]\n"
    "s_cbranch_vccnz LND160_%=\n"
    "LNR160_%=:\n"
    "ds_read_b64_tr_b16 v[224:225], v245 offset:24576\n"
    "ds_read_b64_tr_b16 v[226:227], v245 offset:26624\n"
    "v_exp_f32 v48, v48\n"
    "v_exp_f32 v49, v49\n"
    "v_cvt_pk_bf16_f32 v243, v46, v47\n"
    "v_add_f32 v250, v250, v46\n"
    "v_add_f32 v251, v251, v47\n"
    "s_waitcnt lgkmcnt(14)\n"
    "v_mfma_f32_32x32x16_bf16 v[128:143], v[232:235], v[240:243], v[128:143]\n"
    "ds_read_b64_tr_b16 v[228:229], v246 offset:24576\n"
    "ds_read_b64_tr_b16 v[230:231], v246 offset:26624\n"
    "v_exp_f32 v50, v50\n"
    "v_exp_f32 v51, v51\n"
    "v_cvt_pk_bf16_f32 v236, v48, v49\n"
    "v_add_f32 v250, v250, v48\n"
    "v_add_f32 v251, v251, v49\n"
    "buffer_store_dwordx4 v[192:195], v254, s[48:51], s60 offen sc1\n"
    "s_waitcnt lgkmcnt(14)\n"
    "v_mfma_f32_32x32x16_bf16 v[144:159], v[208:211], v[240:243], v[144:159]\n"
    "ds_read_b64_tr_b16 v[232:233], v247 offset:24576\n"
    "ds_read_b64_tr_b16 v[234:235], v247 offset:26624\n"
    "v_exp_f32 v52, v52\n"
    "v_exp_f32 v53, v53\n"
    "v_cvt_pk_bf16_f32 v237, v50, v51\n"
    "v_add_f32 v250, v250, v50\n"
    "v_add_f32 v251, v251, v51\n"
    "s_waitcnt lgkmcnt(14)\n"
    "v_mfma_f32_32x32x16_bf16 v[160:175], v[212:215], v[240:243], v[160:175]\n"
    "ds_read_b64_tr_b16 v[208:209], v244 offset:28672\n"
    "ds_read_b64_tr_b16 v[210:211], v244 offset:30720\n"
    "v_exp_f32 v54, v54\n"
    "v_exp_f32 v55, v55\n"
    "v_cvt_pk_bf16_f32 v238, v52, v53\n"
    "v_add_f32 v250, v250, v52\n"
    "v_add_f32 v251, v251, v53\n"
    "s_waitcnt lgkmcnt(14)\n"
    "v_mfma_f32_32x32x16_bf16 v[176:191], v[216:219], v[240:243], v[176:191]\n"
    "s_cbranch_vccnz LND161_%=\n"
    "LNR161_%=:\n"
    "ds_read_b64_tr_b16 v[212:213], v245 offset:28672\n"
    "ds_read_b64_tr_b16 v[214:215], v245 offset:30720\n"
    "v_exp_f32 v56, v56\n"
    "v_exp_f32 v57, v57\n"
    "v_cvt_pk_bf16_f32 v239, v54, v55\n"
    "v_add_f32 v250, v250, v54\n"
    "v_add_f32 v251, v251, v55\n"
    "s_waitcnt lgkmcnt(14)\n"
    "v_mfma_f32_32x32x16_bf16 v[128:143], v[220:223], v[236:239], v[128:143]\n"
    "ds_read_b64_tr_b16 v[216:217], v246 offset:28672\n"
    "ds_read_b64_tr_b16 v[218:219], v246 offset:30720\n"
    "v_exp_f32 v58, v58\n"
    "v_exp_f32 v59, v59\n"
    "v_cvt_pk_bf16_f32 v240, v56, v57\n"
    "v_add_f32 v250, v250, v56\n"
    "v_add_f32 v251, v251, v57\n"
    "s_waitcnt lgkmcnt(10)\n"
    "v_mfma_f32_32x32x16_bf16 v[144:159], v[224:227], v[236:239], v[144:159]\n"
    "ds_read_b64_tr_b16 v[220:221], v247 offset:28672\n"
    "ds_read_b64_tr_b16 v[222:223], v247 offset:30720\n"
    "v_exp_f32 v60, v60\n"
    "v_exp_f32 v61, v61\n"
    "v_cvt_pk_bf16_f32 v241, v58, v59\n"
    "v_add_f32 v250, v250, v58\n"
    "v_add_f32 v251, v251, v59\n"
    "buffer_store_dwordx4 v[196:199], v254, s[48:51], s60 offen offset:2048 sc1\n"
    "s_waitcnt lgkmcnt(10)\n"
    "v_mfma_f32_32x32x16_bf16 v[160:175], v[228:231], v[236:239], v[160:175]\n"
    "s_waitcnt vmcnt(4)\n"
    "s_barrier\n"
    "ds_read_b64_tr_b16 v[224:225], v244 offset:32768\n"
    "ds_read_b64_tr_b16 v[226:227], v244 offset:34816\n"
    "v_exp_f32 v62, v62\n"
    "v_exp_f32 v63, v63\n"
    "v_cvt_pk_bf16_f32 v242, v60, v61\n"
    "v_add_f32 v250, v250, v60\n"
    "v_add_f32 v251, v251, v61\n"
    "s_waitcnt lgkmcnt(10)\n"
    "v_mfma_f32_32x32x16_bf16 v[176:191], v[232:235], v[236:239], v[176:191]\n"
    "s_cbranch_vccnz LND162_%=\n"
    "LNR162_%=:\n"
    "ds_read_b64_tr_b16 v[228:229], v245 offset:32768\n"
    "ds_read_b64_tr_b16 v[230:231], v245 offset:34816\n"
    "v_exp_f32 v64, v64\n"
    "v_exp_f32 v65, v65\n"
    "v_cvt_pk_bf16_f32 v243, v62, v63\n"
    "v_add_f32 v250, v250, v62\n"
    "v_add_f32 v251, v251, v63\n"
    "s_waitcnt lgkmcnt(10)\n"
    "v_mfma_f32_32x32x16_bf16 v[128:143], v[208:211], v[240:243], v[128:143]\n"
    "s_memtime s[84:85]\n"
    "s_memrealtime s[86:87]\n"
    "ds_read_b64_tr_b16 v[232:233], v246 offset:32768\n"
    "ds_read_b64_tr_b16 v[234:235], v246 offset:34816\n"
    "v_exp_f32 v66, v66\n"
    "v_exp_f32 v67, v67\n"
    "v_cvt_pk_bf16_f32 v236, v64, v65\n"
    "v_add_f32 v250, v250, v64\n"
    "v_add_f32 v251, v251, v65\n"
    "s_waitcnt lgkmcnt(10)\n"
    "v_mfma_f32_32x32x16_bf16 v[144:159], v[212:215], v[240:243], v[144:159]\n"
    "ds_read_b64_tr_b16 v[208:209], v247 offset:32768\n"
    "ds_read_b64_tr_b16 v[210:211], v247 offset:34816\n"
    "v_exp_f32 v68, v68\n"
    "v_exp_f32 v69, v69\n"
    "v_cvt_pk_bf16_f32 v237, v66, v67\n"
    "v_add_f32 v250, v250, v66\n"
    "v_add_f32 v251, v251, v67\n"
    "s_waitcnt lgkmcnt(10)\n"
    "v_mfma_f32_32x32x16_bf16 v[160:175], v[216:219], v[240:243], v[160:175]\n"
    "ds_read_b64_tr_b16 v[212:213], v244 offset:36864\n"
    "ds_read_b64_tr_b16 v[214:215], v244 offset:38912\n"
    "v_exp_f32 v70, v70\n"
    "v_exp_f32 v71, v71\n"
    "v_cvt_pk_bf16_f32 v238, v68, v69\n"
    "v_add_f32 v250, v250, v68\n"
    "v_add_f32 v251, v251, v69\n"
    "s_add_u32 s63, s60, 4096\n"
    "buffer_store_dwordx4 v[200:203], v254, s[48:51], s63 offen sc1\n"
    "s_waitcnt lgkmcnt(10)\n"
    "v_mfma_f32_32x32x16_bf16 v[176:191], v[220:223], v[240:243], v[176:191]\n"
    "s_cbranch_vccnz LND163_%=\n"
    "LNR163_%=:\n"
    "ds_read_b64_tr_b16 v[216:217], v245 offset:36864\n"
    "ds_read_b64_tr_b16 v[218:219], v245 offset:38912\n"
    "v_exp_f32 v72, v72\n"
    "v_exp_f32 v73, v73\n"
    "v_cvt_pk_bf16_f32 v239, v70, v71\n"
    "v_add_f32 v250, v250, v70\n"
    "v_add_f32 v251, v251, v71\n"
    "s_branch LNX11_%=\n"
    "LND176_%=:\n"
    "s_mov_b32 s91, s89\n"
    "LNL176_%=:\n"
    "s_sub_u32 s91, s91, 1\n"
    "s_cmp_gt_i32 s91, 0\n"
    "s_cbranch_scc1 LNL176_%=\n"
    "s_branch LNR176_%=\n"
    "LND177_%=:\n"
    "s_mov_b32 s91, s89\n"
    "LNL177_%=:\n"
    "s_sub_u32 s91, s91, 1\n"
    "s_cmp_gt_i32 s91, 0\n"
    "s_cbranch_scc1 LNL177_%=\n"
    "s_branch LNR177_%=\n"
    "LND178_%=:\n"
    "s_mov_b32 s91, s89\n"
    "LNL178_%=:\n"
    "s_sub_u32 s91, s91, 1\n"
    "s_cmp_gt_i32 s91, 0\n"
    "s_cbranch_scc1 LNL178_%=\n"
    "s_branch LNR178_%=\n"
    "LND179_%=:\n"
    "s_mov_b32 s91, s89\n"
    "LNL179_%=:\n"
    "s_sub_u32 s91, s91, 1\n"
    "s_cmp_gt_i32 s91, 0\n"
    "s_cbranch_scc1 LNL179_%=\n"
    "s_branch LNR179_%=\n"
    "LNX11_%=:\n"
    "s_waitcnt lgkmcnt(10)\n"
    "v_mfma_f32_32x32x16_bf16 v[128:143], v[224:227], v[236:239], v[128:143]\n"
    "s_sub_u32 s88, s84, s90\n"
    "s_sub_u32 s85, s86, s92\n"
    "s_mov_b32 s90, s84\n"
    "s_mov_b32 s92, s86\n"
    "s_mul_i32 s87, s85, 182\n"
    "s_mul_i32 s91, s88, 10\n"
    "s_cmp_ge_u32 s91, s87\n"
    "s_cselect_b32 s88, s88, 0\n"
    "s_mul_i32 s88, s88, 48\n"
    "s_mov_b32 s89, 0\n"
    "s_mul_i32 s87, s85, 847\n"
    "s_cmp_ge_u32 s88, s87\n"
    "s_addc_u32 s89, s89, 0\n"
    "s_mul_i32 s87, s85, 912\n"
    "s_cmp_ge_u32 s88, s87\n"
    "s_addc_u32 s89, s89, 0\n"
    "s_mul_i32 s87, s85, 977\n"
    "s_cmp_ge_u32 s88, s87\n"
    "s_addc_u32 s89, s89, 0\n"
    "s_mul_i32 s87, s85, 1042\n"
    "s_cmp_ge_u32 s88, s87\n"
    "s_addc_u32 s89, s89, 0\n"
    "s_mul_i32 s87, s85, 1107\n"
    "s_cmp_ge_u32 s88, s87\n"
    "s_addc_u32 s89, s89, 0\n"
    "s_mul_i32 s87, s85, 1172\n"
    "s_cmp_ge_u32 s88, s87\n"
    "s_addc_u32 s89, s89, 0\n"
    "s_cmp_lg_u32 s89, 0\n"
    "s_cselect_b64 vcc, -1, 0\n"
    "ds_read_b64_tr_b16 v[220:221], v246 offset:36864\n"
    "ds_read_b64_tr_b16 v[222:223], v246 offset:38912\n"
    "v_exp_f32 v74, v74\n"
    "v_exp_f32 v75, v75\n"
    "v_cvt_pk_bf16_f32 v240, v72, v73\n"
    "v_add_f32 v250, v250, v72\n"
    "v_add_f32 v251, v251, v73\n"
    "s_waitcnt lgkmcnt(10)\n"
    "v_mfma_f32_32x32x16_bf16 v[144:159], v[228:231], v[236:239], v[144:159]\n"
    "ds_read_b64_tr_b16 v[224:225], v247 offset:36864\n"
    "ds_read_b64_tr_b16 v[226:227], v247 offset:38912\n"
    "v_exp_f32 v76, v76\n"
    "v_exp_f32 v77, v77\n"
    "v_cvt_pk_bf16_f32 v241, v74, v75\n"
    "v_add_f32 v250, v250, v74\n"
    "v_add_f32 v251, v251, v75\n"
    "s_waitcnt lgkmcnt(10)\n"
    "v_mfma_f32_32x32x16_bf16 v[160:175], v[232:235], v[236:239], v[160:175]\n"
    "ds_read_b64_tr_b16 v[228:229], v244 offset:40960\n"
    "ds_read_b64_tr_b16 v[230:231], v244 offset:43008\n"
    "v_exp_f32 v78, v78\n"
    "v_exp_f32 v79, v79\n"
    "v_cvt_pk_bf16_f32 v242, v76, v77\n"
    "v_add_f32 v250, v250, v76\n"
    "v_add_f32 v251, v251, v77\n"
    "s_waitcnt lgkmcnt(10)\n"
    "v_mfma_f32_32x32x16_bf16 v[176:191], v[208:211], v[236:239], v[176:191]\n"
    "s_cbranch_vccnz LND176_%=\n"
    "LNR176_%=:\n"
    "ds_read_b64_tr_b16 v[232:233], v245 offset:40960\n"
    "ds_read_b64_tr_b16 v[234:235], v245 offset:43008\n"
    "v_exp_f32 v80, v80\n"
    "v_exp_f32 v81, v81\n"
    "v_cvt_pk_bf16_f32 v243, v78, v79\n"
    "v_add_f32 v250, v250, v78\n"
    "v_add_f32 v251, v251, v79\n"
    "s_add_u32 s63, s60, 4096\n"
    "buffer_store_dwordx4 v[204:207], v254, s[48:51], s63 offen offset:2048 sc1\n"
    "s_waitcnt lgkmcnt(10)\n"
    "v_mfma_f32_32x32x16_bf16 v[128:143], v[212:215], v[240:243], v[128:143]\n"
    "ds_read_b64_tr_b16 v[208:209], v246 offset:40960\n"
    "ds_read_b64_tr_b16 v[210:211], v246 offset:43008\n"
    "v_exp_f32 v82, v82\n"
    "v_exp_f32 v83, v83\n"
    "v_cvt_pk_bf16_f32 v236, v80, v81\n"
    "v_add_f32 v250, v250, v80\n"
    "v_add_f32 v251, v251, v81\n"
    "s_waitcnt lgkmcnt(10)\n"
    "v_mfma_f32_32x32x16_bf16 v[144:159], v[216:219], v[240:243], v[144:159]\n"
    "ds_read_b64_tr_b16 v[212:213], v247 offset:40960\n"
    "ds_read_b64_tr_b16 v[214:215], v247 offset:43008\n"
    "v_exp_f32 v84, v84\n"
    "v_exp_f32 v85, v85\n"
    "v_cvt_pk_bf16_f32 v237, v82, v83\n"
    "v_add_f32 v250, v250, v82\n"
    "v_add_f32 v251, v251, v83\n"
    "s_waitcnt lgkmcnt(10)\n"
    "v_mfma_f32_32x32x16_bf16 v[160:175], v[220:223], v[240:243], v[160:175]\n"
    "ds_read_b64_tr_b16 v[216:217], v244 offset:45056\n"
    "ds_read_b64_tr_b16 v[218:219], v244 offset:47104\n"
    "v_exp_f32 v86, v86\n"
    "v_exp_f32 v87, v87\n"
    "v_cvt_pk_bf16_f32 v238, v84, v85\n"
    "v_add_f32 v250, v250, v84\n"
    "v_add_f32 v251, v251, v85\n"
    "s_waitcnt lgkmcnt(10)\n"
    "v_mfma_f32_32x32x16_bf16 v[176:191], v[224:227], v[240:243], v[176:191]\n"
    "s_cbranch_vccnz LND177_%=\n"
    "LNR177_%=:\n"
    "ds_read_b64_tr_b16 v[220:221], v245 offset:45056\n"
    "ds_read_b64_tr_b16 v[222:223], v245 offset:47104\n"
    "v_exp_f32 v88, v88\n"
    "v_exp_f32 v89, v89\n"
    "v_cvt_pk_bf16_f32 v239, v86, v87\n"
    "v_add_f32 v250, v250, v86\n"
    "v_add_f32 v251, v251, v87\n"
    "s_waitcnt lgkmcnt(10)\n"
    "v_mfma_f32_32x32x16_bf16 v[128:143], v[228:231], v[236:239], v[128:143]\n"
    "ds_read_b64_tr_b16 v[224:225], v246 offset:45056\n"
    "ds_read_b64_tr_b16 v[226:227], v246 offset:47104\n"
    "v_exp_f32 v90, v90\n"
    "v_exp_f32 v91, v91\n"
    "v_cvt_pk_bf16_f32 v240, v88, v89\n"
    "v_add_f32 v250, v250, v88\n"
    "v_add_f32 v251, v251, v89\n"
    "buffer_store_dwordx4 v[16:19], v254, s[48:51], s60 offen offset:128 sc1\n"
    "s_waitcnt lgkmcnt(10)\n"
    "v_mfma_f32_32x32x16_bf16 v[144:159], v[232:235], v[236:239], v[144:159]\n"
    "ds_read_b64_tr_b16 v[228:229], v247 offset:45056\n"
    "ds_read_b64_tr_b16 v[230:231], v247 offset:47104\n"
    "v_exp_f32 v92, v92\n"
    "v_exp_f32 v93, v93\n"
    "v_cvt_pk_bf16_f32 v241, v90, v91\n"
    "v_add_f32 v250, v250, v90\n"
    "v_add_f32 v251, v251, v91\n"
    "s_waitcnt lgkmcnt(10)\n"
    "v_mfma_f32_32x32x16_bf16 v[160:175], v[208:211], v[236:239], v[160:175]\n"
    "s_waitcnt vmcnt(5)\n"
    "s_barrier\n"
    "ds_read_b64_tr_b16 v[232:233], v244 offset:49152\n"
    "ds_read_b64_tr_b16 v[234:235], v244 offset:51200\n"
    "v_exp_f32 v94, v94\n"
    "v_exp_f32 v95, v95\n"
    "v_cvt_pk_bf16_f32 v242, v92, v93\n"
    "v_add_f32 v250, v250, v92\n"
    "v_add_f32 v251, v251, v93\n"
    "s_waitcnt lgkmcnt(10)\n"
    "v_mfma_f32_32x32x16_bf16 v[176:191], v[212:215], v[236:239], v[176:191]\n"
    "s_cbranch_vccnz LND178_%=\n"
    "LNR178_%=:\n"
    "ds_read_b64_tr_b16 v[208:209], v245 offset:49152\n"
    "ds_read_b64_tr_b16 v[210:211], v245 offset:51200\n"
    "v_exp_f32 v96, v96\n"
    "v_exp_f32 v97, v97\n"
    "v_cvt_pk_bf16_f32 v243, v94, v95\n"
    "v_add_f32 v250, v250, v94\n"
    "v_add_f32 v251, v251, v95\n"
    "s_waitcnt lgkmcnt(10)\n"
    "v_mfma_f32_32x32x16_bf16 v[128:143], v[216:219], v[240:243], v[128:143]\n"
    "s_memtime s[84:85]\n"
    "s_memrealtime s[86:87]\n"
    "ds_read_b64_tr_b16 v[212:213], v246 offset:49152\n"
    "ds_read_b64_tr_b16 v[214:215], v246 offset:51200\n"
    "v_exp_f32 v98, v98\n"
    "v_exp_f32 v99, v99\n"
    "v_cvt_pk_bf16_f32 v236, v96, v97\n"
    "v_add_f32 v250, v250, v96\n"
    "v_add_f32 v251, v251, v97\n"
    "s_waitcnt lgkmcnt(10)\n"
    "v_mfma_f32_32x32x16_bf16 v[144:159], v[220:223], v[240:243], v[144:159]\n"
    "ds_read_b64_tr_b16 v[216:217], v247 offset:49152\n"
    "ds_read_b64_tr_b16 v[218:219], v247 offset:51200\n"
    "v_exp_f32 v100, v100\n"
    "v_exp_f32 v101, v101\n"
    "v_cvt_pk_bf16_f32 v237, v98, v99\n"
    "v_add_f32 v250, v250, v98\n"
    "v_add_f32 v251, v251, v99\n"
    "buffer_store_dwordx4 v[20:23], v254, s[48:51], s60 offen offset:2176 sc1\n"
    "s_waitcnt lgkmcnt(10)\n"
    "v_mfma_f32_32x32x16_bf16 v[160:175], v[224:227], v[240:243], v[160:175]\n"
    "ds_read_b64_tr_b16 v[220:221], v244 offset:53248\n"
    "ds_read_b64_tr_b16 v[222:223], v244 offset:55296\n"
    "v_exp_f32 v102, v102\n"
    "v_exp_f32 v103, v103\n"
    "v_cvt_pk_bf16_f32 v238, v100, v101\n"
    "v_add_f32 v250, v250, v100\n"
    "v_add_f32 v251, v251, v101\n"
    "s_waitcnt lgkmcnt(10)\n"
    "v_mfma_f32_32x32x16_bf16 v[176:191], v[228:231], v[240:243], v[176:191]\n"
    "s_cbranch_vccnz LND179_%=\n"
    "LNR179_%=:\n"
    "ds_read_b64_tr_b16 v[224:225], v245 offset:53248\n"
    "ds_read_b64_tr_b16 v[226:227], v245 offset:55296\n"
    "v_exp_f32 v104, v104\n"
    "v_exp_f32 v105, v105\n"
    "v_cvt_pk_bf16_f32 v239, v102, v103\n"
    "v_add_f32 v250, v250, v102\n"
    "v_add_f32 v251, v251, v103\n"
    "s_branch LNX12_%=\n"
    "LND192_%=:\n"
    "s_mov_b32 s91, s89\n"
    "LNL192_%=:\n"
    "s_sub_u32 s91, s91, 1\n"
    "s_cmp_gt_i32 s91, 0\n"
    "s_cbranch_scc1 LNL192_%=\n"
    "s_branch LNR192_%=\n"
    "LND193_%=:\n"
    "s_mov_b32 s91, s89\n"
    "LNL193_%=:\n"
    "s_sub_u32 s91, s91, 1\n"
    "s_cmp_gt_i32 s91, 0\n"
    "s_cbranch_scc1 LNL193_%=\n"
    "s_branch LNR193_%=\n"
    "LND194_%=:\n"
    "s_mov_b32 s91, s89\n"
    "LNL194_%=:\n"
    "s_sub_u32 s91, s91, 1\n"
    "s_cmp_gt_i32 s91, 0\n"
    "s_cbranch_scc1 LNL194_%=\n"
    "s_branch LNR194_%=\n"
    "LND195_%=:\n"
    "s_mov_b32 s91, s89\n"
    "LNL195_%=:\n"
    "s_sub_u32 s91, s91, 1\n"
    "s_cmp_gt_i32 s91, 0\n"
    "s_cbranch_scc1 LNL195_%=\n"
    "s_branch LNR195_%=\n"
    "LNX12_%=:\n"
    "s_waitcnt lgkmcnt(10)\n"
    "v_mfma_f32_32x32x16_bf16 v[128:143], v[232:235], v[236:239], v[128:143]\n"
    "s_sub_u32 s88, s84, s90\n"
    "s_sub_u32 s85, s86, s92\n"
    "s_mov_b32 s90, s84\n"
    "s_mov_b32 s92, s86\n"
    "s_mul_i32 s87, s85, 182\n"
    "s_mul_i32 s91, s88, 10\n"
    "s_cmp_ge_u32 s91, s87\n"
    "s_cselect_b32 s88, s88, 0\n"
    "s_mul_i32 s88, s88, 48\n"
    "s_mov_b32 s89, 0\n"
    "s_mul_i32 s87, s85, 847\n"
    "s_cmp_ge_u32 s88, s87\n"
    "s_addc_u32 s89, s89, 0\n"
    "s_mul_i32 s87, s85, 912\n"
    "s_cmp_ge_u32 s88, s87\n"
    "s_addc_u32 s89, s89, 0\n"
    "s_mul_i32 s87, s85, 977\n"
    "s_cmp_ge_u32 s88, s87\n"
    "s_addc_u32 s89, s89, 0\n"
    "s_mul_i32 s87, s85, 1042\n"
    "s_cmp_ge_u32 s88, s87\n"
    "s_addc_u32 s89, s89, 0\n"
    "s_mul_i32 s87, s85, 1107\n"
    "s_cmp_ge_u32 s88, s87\n"
    "s_addc_u32 s89, s89, 0\n"
    "s_mul_i32 s87, s85, 1172\n"
    "s_cmp_ge_u32 s88, s87\n"
    "s_addc_u32 s89, s89, 0\n"
    "s_cmp_lg_u32 s89, 0\n"
    "s_cselect_b64 vcc, -1, 0\n"
    "ds_read_b64_tr_b16 v[228:229], v246 offset:53248\n"
    "ds_read_b64_tr_b16 v[230:231], v246 offset:55296\n"
    "v_exp_f32 v106, v106\n"
    "v_exp_f32 v107, v107\n"
    "v_cvt_pk_bf16_f32 v240, v104, v105\n"
    "v_add_f32 v250, v250, v104\n"
    "v_add_f32 v251, v251, v105\n"
    "s_waitcnt lgkmcnt(10)\n"
    "v_mfma_f32_32x32x16_bf16 v[144:159], v[208:211], v[236:239], v[144:159]\n"
    "ds_read_b64_tr_b16 v[232:233], v247 offset:53248\n"
    "ds_read_b64_tr_b16 v[234:235], v247 offset:55296\n"
    "v_exp_f32 v108, v108\n"
    "v_exp_f32 v109, v109\n"
    "v_cvt_pk_bf16_f32 v241, v106, v107\n"
    "v_add_f32 v250, v250, v106\n"
    "v_add_f32 v251, v251, v107\n"
    "s_waitcnt lgkmcnt(10)\n"
    "v_mfma_f32_32x32x16_bf16 v[160:175], v[212:215], v[236:239], v[160:175]\n"
    "ds_read_b64_tr_b16 v[208:209], v244 offset:57344\n"
    "ds_read_b64_tr_b16 v[210:211], v244 offset:59392\n"
    "v_exp_f32 v110, v110\n"
    "v_exp_f32 v111, v111\n"
    "v_cvt_pk_bf16_f32 v242, v108, v109\n"
    "v_add_f32 v250, v250, v108\n"
    "v_add_f32 v251, v251, v109\n"
    "s_add_u32 s63, s60, 4096\n"
    "buffer_store_dwordx4 v[24:27], v254, s[48:51], s63 offen offset:128 sc1\n"
    "s_waitcnt lgkmcnt(10)\n"
    "v_mfma_f32_32x32x16_bf16 v[176:191], v[216:219], v[236:239], v[176:191]\n"
    "s_cbranch_vccnz LND192_%=\n"
    "LNR192_%=:\n"
    "ds_read_b64_tr_b16 v[212:213], v245 offset:57344\n"
    "ds_read_b64_tr_b16 v[214:215], v245 offset:59392\n"
    "v_exp_f32 v112, v112\n"
    "v_exp_f32 v113, v113\n"
    "v_cvt_pk_bf16_f32 v243, v110, v111\n"
    "v_add_f32 v250, v250, v110\n"
    "v_add_f32 v251, v251, v111\n"
    "s_waitcnt lgkmcnt(10)\n"
    "v_mfma_f32_32x32x16_bf16 v[128:143], v[220:223], v[240:243], v[128:143]\n"
    "ds_read_b64_tr_b16 v[216:217], v246 offset:57344\n"
    "ds_read_b64_tr_b16 v[218:219], v246 offset:59392\n"
    "v_exp_f32 v114, v114\n"
    "v_exp_f32 v115, v115\n"
    "v_cvt_pk_bf16_f32 v236, v112, v113\n"
    "v_add_f32 v250, v250, v112\n"
    "v_add_f32 v251, v251, v113\n"
    "s_waitcnt lgkmcnt(10)\n"
    "v_mfma_f32_32x32x16_bf16 v[144:159], v[224:227], v[240:243], v[144:159]\n"
    "ds_read_b64_tr_b16 v[220:221], v247 offset:57344\n"
    "ds_read_b64_tr_b16 v[222:223], v247 offset:59392\n"
    "v_exp_f32 v116, v116\n"
    "v_exp_f32 v117, v117\n"
    "v_cvt_pk_bf16_f32 v237, v114, v115\n"
    "v_add_f32 v250, v250, v114\n"
    "v_add_f32 v251, v251, v115\n"
    "s_waitcnt lgkmcnt(10)\n"
    "v_mfma_f32_32x32x16_bf16 v[160:175], v[228:231], v[240:243], v[160:175]\n"
    "ds_read_b64_tr_b16 v[224:225], v244 offset:61440\n"
    "ds_read_b64_tr_b16 v[226:227], v244 offset:63488\n"
    "v_exp_f32 v118, v118\n"
    "v_exp_f32 v119, v119\n"
    "v_cvt_pk_bf16_f32 v238, v116, v117\n"
    "v_add_f32 v250, v250, v116\n"
    "v_add_f32 v251, v251, v117\n"
    "s_waitcnt lgkmcnt(10)\n"
    "v_mfma_f32_32x32x16_bf16 v[176:191], v[232:235], v[240:243], v[176:191]\n"
    "s_cbranch_vccnz LND193_%=\n"
    "LNR193_%=:\n"
    "ds_read_b64_tr_b16 v[228:229], v245 offset:61440\n"
    "ds_read_b64_tr_b16 v[230:231], v245 offset:63488\n"
    "v_exp_f32 v120, v120\n"
    "v_exp_f32 v121, v121\n"
    "v_cvt_pk_bf16_f32 v239, v118, v119\n"
    "v_add_f32 v250, v250, v118\n"
    "v_add_f32 v251, v251, v119\n"
    "s_add_u32 s63, s60, 4096\n"
    "buffer_store_dwordx4 v[28:31], v254, s[48:51], s63 offen offset:2176 sc1\n"
    "s_waitcnt lgkmcnt(10)\n"
    "v_mfma_f32_32x32x16_bf16 v[128:143], v[208:211], v[236:239], v[128:143]\n"
    "ds_read_b64_tr_b16 v[232:233], v246 offset:61440\n"
    "ds_read_b64_tr_b16 v[234:235], v246 offset:63488\n"
    "v_exp_f32 v122, v122\n"
    "v_exp_f32 v123, v123\n"
    "v_cvt_pk_bf16_f32 v240, v120, v121\n"
    "v_add_f32 v250, v250, v120\n"
    "v_add_f32 v251, v251, v121\n"
    "s_waitcnt lgkmcnt(10)\n"
    "v_mfma_f32_32x32x16_bf16 v[144:159], v[212:215], v[236:239], v[144:159]\n"
    "ds_read_b64_tr_b16 v[208:209], v247 offset:61440\n"
    "ds_read_b64_tr_b16 v[210:211], v247 offset:63488\n"
    "v_exp_f32 v124, v124\n"
    "v_exp_f32 v125, v125\n"
    "v_cvt_pk_bf16_f32 v241, v122, v123\n"
    "v_add_f32 v250, v250, v122\n"
    "v_add_f32 v251, v251, v123\n"
    "s_waitcnt lgkmcnt(10)\n"
    "v_mfma_f32_32x32x16_bf16 v[160:175], v[216:219], v[236:239], v[160:175]\n"
    "v_exp_f32 v126, v126\n"
    "v_exp_f32 v127, v127\n"
    "v_cvt_pk_bf16_f32 v242, v124, v125\n"
    "v_add_f32 v250, v250, v124\n"
    "v_add_f32 v251, v251, v125\n"
    "s_waitcnt lgkmcnt(8)\n"
    "v_mfma_f32_32x32x16_bf16 v[176:191], v[220:223], v[236:239], v[176:191]\n"
    "s_cbranch_vccnz LND194_%=\n"
    "LNR194_%=:\n"
    "v_cvt_pk_bf16_f32 v243, v126, v127\n"
    "v_add_f32 v250, v250, v126\n"
    "v_add_f32 v251, v251, v127\n"
    "s_waitcnt lgkmcnt(6)\n"
    "v_mfma_f32_32x32x16_bf16 v[128:143], v[224:227], v[240:243], v[128:143]\n"
    "v_add_f32 v250, v250, v251\n"
    "v_mov_b32 v251, v250\n"
    "s_nop 1\n"
    "v_permlane32_swap_b32 v250, v251\n"
    "v_add_f32 v250, v250, v251\n"
    "v_cmp_nge_f32 vcc, s57, v250\n"
    "v_rcp_f32 v250, v250\n"
    "s_nop 1\n"
    "s_nop 0\n"
    "s_cmp_lg_u64 vcc, 0\n"
    "s_cselect_b32 s62, 2, 0\n"
    "s_or_b32 s83, s83, s62\n"
    "s_mov_b64 vcc, 0\n"
    "s_waitcnt lgkmcnt(4)\n"
    "v_mfma_f32_32x32x16_bf16 v[144:159], v[228:231], v[240:243], v[144:159]\n"
    "s_waitcnt lgkmcnt(2)\n"
    "v_mfma_f32_32x32x16_bf16 v[160:175], v[232:235], v[240:243], v[160:175]\n"
    "s_waitcnt lgkmcnt(0)\n"
    "v_mfma_f32_32x32x16_bf16 v[176:191], v[208:211], v[240:243], v[176:191]\n"
    "s_cbranch_vccnz LND195_%=\n"
    "LNR195_%=:\n"
    "v_xor_b32 v224, 0, v252\n"
    "v_pk_mul_f32 v[128:129], v[128:129], v[250:251] op_sel_hi:[1,0]\n"
    "v_pk_mul_f32 v[130:131], v[130:131], v[250:251] op_sel_hi:[1,0]\n"
    "v_cvt_pk_bf16_f32 v128, v128, v129\n"
    "v_cvt_pk_bf16_f32 v129, v130, v131\n"
    "ds_write_b64 v224, v[128:129] offset:0\n"
    "v_xor_b32 v225, 16, v252\n"
    "v_pk_mul_f32 v[132:133], v[132:133], v[250:251] op_sel_hi:[1,0]\n"
    "v_pk_mul_f32 v[134:135], v[134:135], v[250:251] op_sel_hi:[1,0]\n"
    "v_cvt_pk_bf16_f32 v132, v132, v133\n"
    "v_cvt_pk_bf16_f32 v133, v134, v135\n"
    "ds_write_b64 v225, v[132:133] offset:0\n"
    "v_xor_b32 v226, 32, v252\n"
    "v_pk_mul_f32 v[136:137], v[136:137], v[250:251] op_sel_hi:[1,0]\n"
    "v_pk_mul_f32 v[138:139], v[138:139], v[250:251] op_sel_hi:[1,0]\n"
    "v_cvt_pk_bf16_f32 v136, v136, v137\n"
    "v_cvt_pk_bf16_f32 v137, v138, v139\n"
    "ds_write_b64 v226, v[136:137] offset:0\n"
    "v_xor_b32 v227, 48, v252\n"
    "v_pk_mul_f32 v[140:141], v[140:141], v[250:251] op_sel_hi:[1,0]\n"
    "v_pk_mul_f32 v[142:143], v[142:143], v[250:251] op_sel_hi:[1,0]\n"
    "v_cvt_pk_bf16_f32 v140, v140, v141\n"
    "v_cvt_pk_bf16_f32 v141, v142, v143\n"
    "ds_write_b64 v227, v[140:141] offset:0\n"
    "v_xor_b32 v224, 64, v252\n"
    "v_pk_mul_f32 v[144:145], v[144:145], v[250:251] op_sel_hi:[1,0]\n"
    "v_pk_mul_f32 v[146:147], v[146:147], v[250:251] op_sel_hi:[1,0]\n"
    "v_cvt_pk_bf16_f32 v144, v144, v145\n"
    "v_cvt_pk_bf16_f32 v145, v146, v147\n"
    "ds_write_b64 v224, v[144:145] offset:0\n"
    "v_xor_b32 v225, 80, v252\n"
    "v_pk_mul_f32 v[148:149], v[148:149], v[250:251] op_sel_hi:[1,0]\n"
    "v_pk_mul_f32 v[150:151], v[150:151], v[250:251] op_sel_hi:[1,0]\n"
    "v_cvt_pk_bf16_f32 v148, v148, v149\n"
    "v_cvt_pk_bf16_f32 v149, v150, v151\n"
    "ds_write_b64 v225, v[148:149] offset:0\n"
    "v_xor_b32 v226, 96, v252\n"
    "v_pk_mul_f32 v[152:153], v[152:153], v[250:251] op_sel_hi:[1,0]\n"
    "v_pk_mul_f32 v[154:155], v[154:155], v[250:251] op_sel_hi:[1,0]\n"
    "v_cvt_pk_bf16_f32 v152, v152, v153\n"
    "v_cvt_pk_bf16_f32 v153, v154, v155\n"
    "ds_write_b64 v226, v[152:153] offset:0\n"
    "v_xor_b32 v227, 112, v252\n"
    "v_pk_mul_f32 v[156:157], v[156:157], v[250:251] op_sel_hi:[1,0]\n"
    "v_pk_mul_f32 v[158:159], v[158:159], v[250:251] op_sel_hi:[1,0]\n"
    "v_cvt_pk_bf16_f32 v156, v156, v157\n"
    "v_cvt_pk_bf16_f32 v157, v158, v159\n"
    "ds_write_b64 v227, v[156:157] offset:0\n"
    "ds_read_b128 v[32:35], v249 offset:0\n"
    "ds_read_b128 v[36:39], v249 offset:1024\n"
    "ds_read_b128 v[40:43], v249 offset:2048\n"
    "ds_read_b128 v[44:47], v249 offset:3072\n"
    "s_waitcnt lgkmcnt(3)\n"
    "buffer_store_dwordx4 v[32:35], v254, s[48:51], s61 offen sc1\n"
    "s_waitcnt lgkmcnt(2)\n"
    "buffer_store_dwordx4 v[36:39], v254, s[48:51], s61 offen offset:2048 sc1\n"
    "s_add_u32 s63, s61, 4096\n"
    "s_waitcnt lgkmcnt(1)\n"
    "buffer_store_dwordx4 v[40:43], v254, s[48:51], s63 offen sc1\n"
    "s_add_u32 s63, s61, 4096\n"
    "s_waitcnt lgkmcnt(0)\n"
    "buffer_store_dwordx4 v[44:47], v254, s[48:51], s63 offen offset:2048 sc1\n"
    "v_xor_b32 v224, 0, v252\n"
    "v_pk_mul_f32 v[160:161], v[160:161], v[250:251] op_sel_hi:[1,0]\n"
    "v_pk_mul_f32 v[162:163], v[162:163], v[250:251] op_sel_hi:[1,0]\n"
    "v_cvt_pk_bf16_f32 v160, v160, v161\n"
    "v_cvt_pk_bf16_f32 v161, v162, v163\n"
    "ds_write_b64 v224, v[160:161] offset:0\n"
    "v_xor_b32 v225, 16, v252\n"
    "v_pk_mul_f32 v[164:165], v[164:165], v[250:251] op_sel_hi:[1,0]\n"
    "v_pk_mul_f32 v[166:167], v[166:167], v[250:251] op_sel_hi:[1,0]\n"
    "v_cvt_pk_bf16_f32 v164, v164, v165\n"
    "v_cvt_pk_bf16_f32 v165, v166, v167\n"
    "ds_write_b64 v225, v[164:165] offset:0\n"
    "v_xor_b32 v226, 32, v252\n"
    "v_pk_mul_f32 v[168:169], v[168:169], v[250:251] op_sel_hi:[1,0]\n"
    "v_pk_mul_f32 v[170:171], v[170:171], v[250:251] op_sel_hi:[1,0]\n"
    "v_cvt_pk_bf16_f32 v168, v168, v169\n"
    "v_cvt_pk_bf16_f32 v169, v170, v171\n"
    "ds_write_b64 v226, v[168:169] offset:0\n"
    "v_xor_b32 v227, 48, v252\n"
    "v_pk_mul_f32 v[172:173], v[172:173], v[250:251] op_sel_hi:[1,0]\n"
    "v_pk_mul_f32 v[174:175], v[174:175], v[250:251] op_sel_hi:[1,0]\n"
    "v_cvt_pk_bf16_f32 v172, v172, v173\n"
    "v_cvt_pk_bf16_f32 v173, v174, v175\n"
    "ds_write_b64 v227, v[172:173] offset:0\n"
    "v_xor_b32 v224, 64, v252\n"
    "v_pk_mul_f32 v[176:177], v[176:177], v[250:251] op_sel_hi:[1,0]\n"
    "v_pk_mul_f32 v[178:179], v[178:179], v[250:251] op_sel_hi:[1,0]\n"
    "v_cvt_pk_bf16_f32 v176, v176, v177\n"
    "v_cvt_pk_bf16_f32 v177, v178, v179\n"
    "ds_write_b64 v224, v[176:177] offset:0\n"
    "v_xor_b32 v225, 80, v252\n"
    "v_pk_mul_f32 v[180:181], v[180:181], v[250:251] op_sel_hi:[1,0]\n"
    "v_pk_mul_f32 v[182:183], v[182:183], v[250:251] op_sel_hi:[1,0]\n"
    "v_cvt_pk_bf16_f32 v180, v180, v181\n"
    "v_cvt_pk_bf16_f32 v181, v182, v183\n"
    "ds_write_b64 v225, v[180:181] offset:0\n"
    "v_xor_b32 v226, 96, v252\n"
    "v_pk_mul_f32 v[184:185], v[184:185], v[250:251] op_sel_hi:[1,0]\n"
    "v_pk_mul_f32 v[186:187], v[186:187], v[250:251] op_sel_hi:[1,0]\n"
    "v_cvt_pk_bf16_f32 v184, v184, v185\n"
    "v_cvt_pk_bf16_f32 v185, v186, v187\n"
    "ds_write_b64 v226, v[184:185] offset:0\n"
    "v_xor_b32 v227, 112, v252\n"
    "v_pk_mul_f32 v[188:189], v[188:189], v[250:251] op_sel_hi:[1,0]\n"
    "v_pk_mul_f32 v[190:191], v[190:191], v[250:251] op_sel_hi:[1,0]\n"
    "v_cvt_pk_bf16_f32 v188, v188, v189\n"
    "v_cvt_pk_bf16_f32 v189, v190, v191\n"
    "ds_write_b64 v227, v[188:189] offset:0\n"
    "ds_read_b128 v[48:51], v249 offset:0\n"
    "ds_read_b128 v[52:55], v249 offset:1024\n"
    "ds_read_b128 v[56:59], v249 offset:2048\n"
    "ds_read_b128 v[60:63], v249 offset:3072\n"
    "s_waitcnt lgkmcnt(3)\n"
    "buffer_store_dwordx4 v[48:51], v254, s[48:51], s61 offen offset:128 sc1\n"
    "s_waitcnt lgkmcnt(2)\n"
    "buffer_store_dwordx4 v[52:55], v254, s[48:51], s61 offen offset:2176 sc1\n"
    "s_add_u32 s63, s61, 4096\n"
    "s_waitcnt lgkmcnt(1)\n"
    "buffer_store_dwordx4 v[56:59], v254, s[48:51], s63 offen offset:128 sc1\n"
    "s_add_u32 s63, s61, 4096\n"
    "s_waitcnt lgkmcnt(0)\n"
    "buffer_store_dwordx4 v[60:63], v254, s[48:51], s63 offen offset:2176 sc1\n"
    "s_mov_b32 %0, s83\n"
    : "=s"(flag)
    : "s"(__builtin_amdgcn_kernarg_segment_ptr()), "s"(bid), "s"(wid)
    : "memory", "scc", "vcc", "v0", "v1", "v2", "v3", "v4", "v5", "v6", "v7", "v8", "v9", "v10", "v11", "v12", "v13", "v14", "v15", "v16", "v17", "v18", "v19", "v20", "v21", "v22", "v23", "v24", "v25", "v26", "v27", "v28", "v29", "v30", "v31", "v32", "v33", "v34", "v35", "v36", "v37", "v38", "v39", "v40", "v41", "v42", "v43", "v44", "v45", "v46", "v47", "v48", "v49", "v50", "v51", "v52", "v53", "v54", "v55", "v56", "v57", "v58", "v59", "v60", "v61", "v62", "v63", "v64", "v65", "v66", "v67", "v68", "v69", "v70", "v71", "v72", "v73", "v74", "v75", "v76", "v77", "v78", "v79", "v80", "v81", "v82", "v83", "v84", "v85", "v86", "v87", "v88", "v89", "v90", "v91", "v92", "v93", "v94", "v95", "v96", "v97", "v98", "v99", "v100", "v101", "v102", "v103", "v104", "v105", "v106", "v107", "v108", "v109", "v110", "v111", "v112", "v113", "v114", "v115", "v116", "v117", "v118", "v119", "v120", "v121", "v122", "v123", "v124", "v125", "v126", "v127", "v128", "v129", "v130", "v131", "v132", "v133", "v134", "v135", "v136", "v137", "v138", "v139", "v140", "v141", "v142", "v143", "v144", "v145", "v146", "v147", "v148", "v149", "v150", "v151", "v152", "v153", "v154", "v155", "v156", "v157", "v158", "v159", "v160", "v161", "v162", "v163", "v164", "v165", "v166", "v167", "v168", "v169", "v170", "v171", "v172", "v173", "v174", "v175", "v176", "v177", "v178", "v179", "v180", "v181", "v182", "v183", "v184", "v185", "v186", "v187", "v188", "v189", "v190", "v191", "v192", "v193", "v194", "v195", "v196", "v197", "v198", "v199", "v200", "v201", "v202", "v203", "v204", "v205", "v206", "v207", "v208", "v209", "v210", "v211", "v212", "v213", "v214", "v215", "v216", "v217", "v218", "v219", "v220", "v221", "v222", "v223", "v224", "v225", "v226", "v227", "v228", "v229", "v230", "v231", "v232", "v233", "v234", "v235", "v236", "v237", "v238", "v239", "v240", "v241", "v242", "v243", "v244", "v245", "v246", "v247", "v248", "v249", "v250", "v251", "v252", "v253", "v254", "v255", "s36", "s37", "s38", "s39", "s40", "s41", "s42", "s43", "s44", "s45", "s46", "s47", "s48", "s49", "s50", "s51", "s52", "s53", "s54", "s55", "s56", "s57", "s58", "s59", "s60", "s61", "s62", "s63", "s64", "s65", "s66", "s67", "s68", "s69", "s70", "s71", "s72", "s73", "s74", "s75", "s76", "s77", "s78", "s79", "s80", "s81", "s82", "s83", "s84", "s85", "s86", "s87", "s88", "s89", "s90", "s91", "s92", "s93", "s94", "s95", "s96", "s97", "s98", "s99");
  if (flag) {
    asm volatile("s_waitcnt vmcnt(0)" ::: "memory");
    const unsigned lane = __builtin_amdgcn_mbcnt_hi(~0u, __builtin_amdgcn_mbcnt_lo(~0u, 0u));
#pragma unroll 1
    for (int hh = 0; hh < 2; ++hh)
      if (flag & (1u << hh)) {
        const size_t hb = (size_t)(bid + 256u * hh) * (256 * 128), rb = hb + (size_t)wid * (32 * 128);
        exact_rows((const unsigned short*)Q + rb, (const unsigned short*)K + hb, (const unsigned short*)V + hb, (unsigned short*)O + rb, lane);
      }
  }
}

extern "C" void kernel_launch(void* const* d_in, const int* in_sizes, int n_in, void* d_out, int out_size, void* d_ws, size_t ws_size, hipStream_t stream) {
  (void)hipFuncSetAttribute((const void*)mha_fwd, hipFuncAttributeMaxDynamicSharedMemorySize, 163840);
  hipLaunchKernelGGL(mha_fwd, dim3(256), dim3(512), 163840, stream, d_in[0], d_in[1], d_in[2], d_out);
}
```
